```python
import torch

def _softmax(x, axis=-1):
    return torch.softmax(x, dim=axis)
def _gelu(x):
    return torch.nn.functional.gelu(x, approximate="tanh")
def _silu(x):
    return torch.nn.functional.silu(x)
def _sigmoid(x):
    return torch.sigmoid(x)
def _layernorm(x, axis=-1, eps=1e-5):
    m = x.mean(dim=axis, keepdim=True)
    v = x.var(dim=axis, keepdim=True, unbiased=False)
    return (x - m) / torch.sqrt(v + eps)
def _scatter_add(tgt, idx, src, axis=0):
    return tgt.clone().index_add_(axis, idx, src)
def _segment_sum(src, seg, n):
    out = torch.zeros((n,) + src.shape[1:], dtype=src.dtype)
    return out.index_add_(0, seg, src)
def _pad(x, pw):
    flat = tuple(v for pair in reversed(pw) for v in pair)
    return torch.nn.functional.pad(x, flat)

kernel_name = 'concat_heads__ssm_conv_h3__linear_attn_block__prompt'

def setup_inputs():

    _w = (lambda a: (lambda t: t.to(torch.bfloat16) if t.is_floating_point() else t)(torch.as_tensor(a)))
    def _A_setup():
        B, S, D = 1, 2048, 640
        ks = 4
        return {
            "q": _w((lambda *s: torch.randn(*s, dtype=torch.bfloat16) * 0.05)(B, S, D)),
            "k": _w((lambda *s: torch.randn(*s, dtype=torch.bfloat16) * 0.4)(B, S, D)),
            "v": _w((lambda *s: torch.randn(*s, dtype=torch.bfloat16) * 0.4)(B, S, D)),
            "shift_k": _w((lambda *s: torch.randn(*s, dtype=torch.bfloat16) * 0.4)(D, ks) * 0.5),
            "ssm_k": _w((lambda *s: torch.randn(*s, dtype=torch.bfloat16) * 0.4)(D, S) * (1.0 / S ** 0.5)),
            "ssm_D": _w((lambda *s: torch.randn(*s, dtype=torch.bfloat16) * 0.4)(D)),
        }
    _A_in = _A_setup()

    def _B_setup():
        B, S, D = 1, 2048, 640
        return {
            "x": _w((lambda *s: torch.randn(*s, dtype=torch.bfloat16) * 0.05)(B, S, D)),
            "wqkv": _w((lambda *s: torch.randn(*s, dtype=torch.bfloat16) * 0.8)(D, 3 * D)),
            "wo": _w((lambda *s: torch.randn(*s, dtype=torch.bfloat16) * 0.05)(D, D)),
        }
    _B_in = _B_setup()

    return {
        "A_q": _A_in["q"],
        "A_k": _A_in["k"],
        "A_v": _A_in["v"],
        "A_shift_k": _A_in["shift_k"],
        "A_ssm_k": _A_in["ssm_k"],
        "A_ssm_D": _A_in["ssm_D"],
        "B_x": _B_in["x"],
        "B_wqkv": _B_in["wqkv"],
        "B_wo": _B_in["wo"],
    }

def reference(A_q, A_k, A_v, A_shift_k, A_ssm_k, A_ssm_D, B_x, B_wqkv, B_wo):
    def _A_ref(q, k, v, shift_k, ssm_k, ssm_D):
        B, S, D = q.shape
        ks = shift_k.shape[-1]
        k_pad = _pad(k, ((0, 0), (ks - 1, 0), (0, 0)))
        k_shift = torch.zeros_like(k)
        for t in range(ks):
            k_shift = k_shift + k_pad[:, t:t + S, :] * shift_k[:, ks - 1 - t][None, None, :]
        kv = k_shift * v
        L = 2 * S
        kv_pad = _pad(kv, ((0, 0), (0, S), (0, 0)))
        kf_pad = _pad(ssm_k, ((0, 0), (0, S)))
        kv_f = (lambda x, axes=(-2,-1): torch.fft.rfft2(x.float(), dim=axes))(kv_pad[:, None, :, :], axes=(1, 2))
        kf_f = (lambda x, axes=(-2,-1): torch.fft.rfft2(x.float(), dim=axes))(kf_pad[:, None, :], axes=(1, 2))
        kf_f = (lambda x, *d: x.permute(*d))(kf_f, 1, 2, 0)
        y_f = kv_f * kf_f[None, :, :, :]
        y = (lambda x, s, axes=(-2,-1): torch.fft.irfft2(x, s=s, dim=axes).to(torch.bfloat16))(y_f, (1, L), axes=(1, 2))[:, 0, :S, :]
        y = y + kv * ssm_D[None, None, :]
        return q * y

    def _B_ref(x, wqkv, wo):
        B, S, D = x.shape
        H = 32; Dh = D // H
        qkv = (lambda x, *d: x.permute(*d))((x @ wqkv).reshape(B, S, 3, H, Dh), 2, 0, 3, 1, 4)
        q, k, v = qkv[0], qkv[1], qkv[2]
        phi = lambda t: torch.where(t > 0, t + 1, torch.exp(t)) + 1e-6
        pq, pk = phi(q), phi(k)
        kv = torch.einsum("bhsd,bhse->bhde", pk, v)
        z = torch.einsum("bhsd->bhd", pk)
        num = torch.einsum("bhqd,bhde->bhqe", pq, kv)
        den = torch.einsum("bhqd,bhd->bhq", pq, z)[..., None] + 1e-6
        o = num / den
        return (lambda x, *d: x.permute(*d))(o, 0, 2, 1, 3).reshape(B, S, D) @ wo

    a = _A_ref(A_q, A_k, A_v, A_shift_k, A_ssm_k, A_ssm_D)
    b = _B_ref(B_x, B_wqkv, B_wo)
    return torch.concatenate([a, b], axis=-1)
```

```cpp
#include <hip/hip_runtime.h>
#include <hip/hip_bf16.h>
typedef unsigned short u16;
__device__ __forceinline__ float b2f(u16 x){ return __uint_as_float(((unsigned)x)<<16); }
__device__ __forceinline__ u16 f2b(float f){ unsigned u=__float_as_uint(f); u += 0x7fffu + ((u>>16)&1u); return (u16)(u>>16); }
__device__ __forceinline__ float rb(float f){ return b2f(f2b(f)); }
constexpr int S=2048, D=640, H=32, DH=20;

__global__ void k_kv(const u16* k,const u16* v,const u16* sk,u16* kvT){
  int i=blockIdx.x*256+threadIdx.x; if(i>=S*D) return; int s=i/D,d=i%D;
  float acc=0.f;
  for(int t=0;t<4;t++){ int ss=s-3+t; float kk= ss>=0? b2f(k[ss*D+d]):0.f; acc=rb(acc+rb(kk*b2f(sk[d*4+3-t]))); }
  kvT[d*S+s]=f2b(acc*b2f(v[i]));
}
__global__ void k_conv(const u16* kvT,const u16* h,const u16* Dv,const u16* q,u16* out){
  __shared__ float xs[S], hs[S];
  int d=blockIdx.x;
  for(int i=threadIdx.x;i<S;i+=256){ xs[i]=b2f(kvT[d*S+i]); hs[i]=b2f(h[d*S+i]); }
  __syncthreads();
  for(int s=threadIdx.x;s<S;s+=256){
    float acc=0.f; for(int j=0;j<=s;j++) acc+=xs[j]*hs[s-j];
    float y=rb(rb(acc)+rb(xs[s]*b2f(Dv[d])));
    out[s*1280+d]=f2b(b2f(q[s*D+d])*y);
  }
}
__global__ void k_qkv(const u16* x,const u16* w,u16* qkv){
  int n=blockIdx.x*64+(threadIdx.x&63); int s=blockIdx.y*4+(threadIdx.x>>6);
  float acc=0.f; for(int k=0;k<D;k++) acc+=b2f(x[s*D+k])*b2f(w[k*1920+n]);
  float t=rb(acc);
  if(n<1280){ float p= t>0? rb(t+1.f): rb(__expf(t)); t=rb(p+1e-6f); }
  qkv[s*1920+n]=f2b(t);
}
__global__ void k_state(const u16* qkv,float* st){
  int h=blockIdx.x; int i=threadIdx.x; if(i>=420) return; int d=i/21,e=i%21;
  float acc=0.f; for(int s=0;s<S;s++){ float pk=b2f(qkv[s*1920+640+h*20+d]); float vv= e<20? b2f(qkv[s*1920+1280+h*20+e]):1.f; acc+=pk*vv; }
  st[h*420+i]=rb(acc);
}
__global__ void k_o(const u16* qkv,const float* st,u16* o){
  int i=blockIdx.x*256+threadIdx.x; if(i>=S*D) return; int s=i/D,c=i%D,h=c/20,e=c%20;
  float num=0.f,den=0.f; for(int d=0;d<20;d++){ float p=b2f(qkv[s*1920+h*20+d]); num+=p*st[h*420+d*21+e]; den+=p*st[h*420+d*21+20]; }
  o[i]=f2b(rb(num)/rb(rb(den)+1e-6f));
}
__global__ void k_out(const u16* o,const u16* w,u16* out){
  int n=blockIdx.x*64+(threadIdx.x&63); int s=blockIdx.y*4+(threadIdx.x>>6);
  float acc=0.f; for(int k=0;k<D;k++) acc+=b2f(o[s*D+k])*b2f(w[k*D+n]);
  out[s*1280+640+n]=f2b(acc);
}
extern "C" void kernel_launch(void* const* d_in, const int* in_sizes, int n_in, void* d_out, int out_size, void* d_ws, size_t ws_size, hipStream_t stream){
  const u16 *q=(const u16*)d_in[0],*k=(const u16*)d_in[1],*v=(const u16*)d_in[2],*sk=(const u16*)d_in[3],*h=(const u16*)d_in[4],*Dv=(const u16*)d_in[5],*x=(const u16*)d_in[6],*wqkv=(const u16*)d_in[7],*wo=(const u16*)d_in[8];
  u16* out=(u16*)d_out; char* ws=(char*)d_ws;
  u16* kvT=(u16*)ws; u16* qkv=(u16*)(ws+(4<<20)); float* st=(float*)(ws+(16<<20)); u16* o=(u16*)(ws+(20<<20));
  k_kv<<<S*D/256,256,0,stream>>>(k,v,sk,kvT);
  k_conv<<<D,256,0,stream>>>(kvT,h,Dv,q,out);
  k_qkv<<<dim3(30,S/4),256,0,stream>>>(x,wqkv,qkv);
  k_state<<<H,448,0,stream>>>(qkv,st);
  k_o<<<S*D/256,256,0,stream>>>(qkv,st,o);
  k_out<<<dim3(10,S/4),256,0,stream>>>(o,wo,out);
}
```

```cpp
#include <hip/hip_runtime.h>
#include <hip/hip_bf16.h>
typedef unsigned short u16;
__device__ __forceinline__ float b2f(u16 x){ return __uint_as_float(((unsigned)x)<<16); }
__device__ __forceinline__ u16 f2b(float f){ unsigned u=__float_as_uint(f); u += 0x7fffu + ((u>>16)&1u); return (u16)(u>>16); }
__device__ __forceinline__ float rb(float f){ return b2f(f2b(f)); }
constexpr int S=2048, D=640, H=32, DH=20;

__global__ void k_kv(const u16* k,const u16* v,const u16* sk,u16* kvT){
  int i=blockIdx.x*256+threadIdx.x; if(i>=S*D) return; int s=i/D,d=i%D;
  float acc=0.f;
  for(int t=0;t<4;t++){ int ss=s-3+t; float kk= ss>=0? b2f(k[ss*D+d]):0.f; acc=rb(acc+rb(kk*b2f(sk[d*4+3-t]))); }
  kvT[d*S+s]=f2b(acc*b2f(v[i]));
}
__global__ void k_conv(const u16* kvT,const u16* h,const u16* Dv,const u16* q,u16* out){
  __shared__ float xs[S], hs[S];
  int d=blockIdx.x;
  for(int i=threadIdx.x;i<S;i+=256){ xs[i]=b2f(kvT[d*S+i]); hs[i]=b2f(h[d*S+i]); }
  __syncthreads();
  for(int s=threadIdx.x;s<S;s+=256){
    float acc=0.f; for(int j=0;j<=s;j++) acc+=xs[j]*hs[s-j];
    float y=rb(rb(acc)+rb(xs[s]*b2f(Dv[d])));
    out[s*1280+d]=f2b(b2f(q[s*D+d])*y);
  }
}
__global__ void k_qkv(const u16* x,const u16* w,u16* qkv){
  int n=blockIdx.x*64+(threadIdx.x&63); int s=blockIdx.y*4+(threadIdx.x>>6);
  float acc=0.f; for(int k=0;k<D;k++) acc+=b2f(x[s*D+k])*b2f(w[k*1920+n]);
  float t=rb(acc);
  if(n<1280){ float p= t>0? rb(t+1.f): rb(__expf(t)); t=rb(p+1e-6f); }
  qkv[s*1920+n]=f2b(t);
}
__global__ void k_state(const u16* qkv,float* st){
  int h=blockIdx.x; int i=threadIdx.x; if(i>=420) return; int d=i/21,e=i%21;
  float acc=0.f; for(int s=0;s<S;s++){ float pk=b2f(qkv[s*1920+640+h*20+d]); float vv= e<20? b2f(qkv[s*1920+1280+h*20+e]):1.f; acc+=pk*vv; }
  st[h*420+i]=rb(acc);
}
__global__ void k_o(const u16* qkv,const float* st,u16* o){
  int i=blockIdx.x*256+threadIdx.x; if(i>=S*D) return; int s=i/D,c=i%D,h=c/20,e=c%20;
  float num=0.f,den=0.f; for(int d=0;d<20;d++){ float p=b2f(qkv[s*1920+h*20+d]); num+=p*st[h*420+d*21+e]; den+=p*st[h*420+d*21+20]; }
  o[i]=f2b(rb(num)/rb(rb(den)+1e-6f));
}
__global__ void k_out(const u16* o,const u16* w,u16* out){
  int n=blockIdx.x*64+(threadIdx.x&63); int s=blockIdx.y*4+(threadIdx.x>>6);
  float acc=0.f; for(int k=0;k<D;k++) acc+=b2f(o[s*D+k])*b2f(w[k*D+n]);
  out[s*1280+640+n]=f2b(acc);
}
#define MODE 4
#define WHO -1

typedef unsigned v4u __attribute__((ext_vector_type(4)));
typedef unsigned long long u64;
__device__ __forceinline__ u64 rt(){ return __builtin_amdgcn_s_memrealtime(); }
__global__ void __launch_bounds__(512) k_probe(const char* q,const char* k,const char* v,const char* h,const char* x,const char* w,const char* wo,unsigned* sink){
  int b=blockIdx.x,t=threadIdx.x; u64 t0=rt(); unsigned acc=0;
  if(acc==0x12345678) sink[t]=acc;
  __syncthreads();
  u64 t1=rt();
  u64 until=t1+100*(t1-t0);
  if(WHO<0 || b==WHO) while(rt()<until) __builtin_amdgcn_s_sleep(4);
}
extern "C" void kernel_launch(void* const* d_in, const int* in_sizes, int n_in, void* d_out, int out_size, void* d_ws, size_t ws_size, hipStream_t stream){
  const u16 *q=(const u16*)d_in[0],*k=(const u16*)d_in[1],*v=(const u16*)d_in[2],*sk=(const u16*)d_in[3],*h=(const u16*)d_in[4],*Dv=(const u16*)d_in[5],*x=(const u16*)d_in[6],*wqkv=(const u16*)d_in[7],*wo=(const u16*)d_in[8];
  u16* out=(u16*)d_out; char* ws=(char*)d_ws;
  u16* kvT=(u16*)ws; u16* qkv=(u16*)(ws+(4<<20)); float* st=(float*)(ws+(16<<20)); u16* o=(u16*)(ws+(20<<20));
  k_probe<<<256,512,0,stream>>>((const char*)q,(const char*)k,(const char*)v,(const char*)h,(const char*)x,(const char*)wqkv,(const char*)wo,(unsigned*)(ws+(30<<20)));
  k_kv<<<S*D/256,256,0,stream>>>(k,v,sk,kvT);
  k_conv<<<D,256,0,stream>>>(kvT,h,Dv,q,out);
  k_qkv<<<dim3(30,S/4),256,0,stream>>>(x,wqkv,qkv);
  k_state<<<H,448,0,stream>>>(qkv,st);
  k_o<<<S*D/256,256,0,stream>>>(qkv,st,o);
  k_out<<<dim3(10,S/4),256,0,stream>>>(o,wo,out);
}
```

```cpp
#include <hip/hip_runtime.h>
#include <hip/hip_bf16.h>

typedef unsigned short u16;
typedef unsigned long long u64;
typedef short s16x4 __attribute__((ext_vector_type(4)));
typedef short s16x8 __attribute__((ext_vector_type(8)));
typedef __bf16 bf16x2 __attribute__((ext_vector_type(2)));
typedef __bf16 bf16x8 __attribute__((ext_vector_type(8)));
typedef float f32x2 __attribute__((ext_vector_type(2)));
typedef float f32x4 __attribute__((ext_vector_type(4)));
typedef unsigned u32x2 __attribute__((ext_vector_type(2)));
typedef unsigned u32x4 __attribute__((ext_vector_type(4)));
typedef int i32x4 __attribute__((ext_vector_type(4)));
#define LAS __attribute__((address_space(3)))
#define GAS __attribute__((address_space(1)))
#define DI __device__ __forceinline__
#define WAITV(n) asm volatile("s_waitcnt vmcnt(" #n ")" ::: "memory")
#define WAITL0() __builtin_amdgcn_s_waitcnt(0xC07F)
#define BAR() do { asm volatile("" ::: "memory"); __builtin_amdgcn_s_barrier(); asm volatile("" ::: "memory"); } while (0)
#define SCHED() __builtin_amdgcn_sched_barrier(0)
#define ROT 1
#define STAMP 0

DI f32x4 mfma16(u32x4 a, u32x4 b, f32x4 c) { return __builtin_amdgcn_mfma_f32_16x16x32_bf16(__builtin_bit_cast(bf16x8, a), __builtin_bit_cast(bf16x8, b), c, 0, 0, 0); }
DI u32x4 cat(u32x2 a, u32x2 b) { return __builtin_shufflevector(a, b, 0, 1, 2, 3); }
DI u32x4 ld128(unsigned a, int off) { return *(const LAS u32x4*)((LAS char*)(size_t)a + off); }
DI u32x2 ld64(unsigned a, int off) { return *(const LAS u32x2*)((LAS char*)(size_t)a + off); }
DI u32x2 ldtr(unsigned a, int off) { return __builtin_bit_cast(u32x2, __builtin_amdgcn_ds_read_tr16_b64_v4i16((LAS s16x4*)((LAS char*)(size_t)a + off))); }
DI void st128(unsigned a, int off, u32x4 v) { *(LAS u32x4*)((LAS char*)(size_t)a + off) = v; }
DI void st64(unsigned a, int off, u32x2 v) { *(LAS u32x2*)((LAS char*)(size_t)a + off) = v; }
DI void st16(unsigned a, int off, u16 v) { *(LAS u16*)((LAS char*)(size_t)a + off) = v; }
DI unsigned pk(float lo, float hi) { f32x2 v = {lo, hi}; return __builtin_bit_cast(unsigned, __builtin_convertvector(v, bf16x2)); }
DI float blo(unsigned u) { return __uint_as_float(u << 16); }
DI float bhi(unsigned u) { return __uint_as_float(u & 0xffff0000u); }
DI float phi(float t) { return __builtin_fmaxf(t, 0.f) + __builtin_amdgcn_exp2f(__builtin_fminf(t, 0.f) * 1.44269504088896341f) + 1e-6f; }
DI i32x4 make_srd(const void* p, unsigned bytes) { u64 a = (u64)p; i32x4 r; r[0] = (int)(unsigned)a; r[1] = (int)(unsigned)(a >> 32) & 0xffff; r[2] = (int)bytes; r[3] = 0x00020000; return r; }
DI void dma16(i32x4 srd, unsigned voff, unsigned soff, unsigned m0v) {
  asm volatile("s_mov_b32 m0, %3\n\ts_nop 0\n\tbuffer_load_dwordx4 %0, %1, %2 offen lds" ::"v"(voff), "s"(srd), "s"(soff), "s"(m0v) : "memory");
}
typedef __amdgpu_buffer_rsrc_t rsrc_t;
DI rsrc_t mk(const void* p, unsigned bytes) { return __builtin_amdgcn_make_buffer_rsrc((void*)p, 0, bytes, 0x00020000); }
constexpr int SC1 = 16;
DI u32x4 gld128(rsrc_t r, unsigned v, unsigned s) { return __builtin_amdgcn_raw_buffer_load_b128(r, v, s, 0); }
DI u32x4 gld128c(rsrc_t r, unsigned v, unsigned s) { return __builtin_amdgcn_raw_buffer_load_b128(r, v, s, SC1); }
DI u32x2 gld64c(rsrc_t r, unsigned v, unsigned s) { return __builtin_amdgcn_raw_buffer_load_b64(r, v, s, SC1); }
DI void gst128c(rsrc_t r, unsigned v, unsigned s, u32x4 d) { __builtin_amdgcn_raw_buffer_store_b128(d, r, v, s, SC1); }
DI void gst64c(rsrc_t r, unsigned v, unsigned s, u32x2 d) { __builtin_amdgcn_raw_buffer_store_b64(d, r, v, s, SC1); }

constexpr unsigned WS_EPOCH = 0;
constexpr unsigned WS_FLAG = 4096;
constexpr unsigned FL_P = 0;
constexpr unsigned FL_O = 512;
constexpr unsigned FL_K = 1024;
constexpr unsigned FL_Y = 1152;
constexpr unsigned WS_PART = 1u << 18;
constexpr unsigned WS_O = 1u << 20;
constexpr unsigned WS_KVT = 4u << 20;
constexpr unsigned WS_YT = 8u << 20;
constexpr unsigned WS_DBG = 12u << 20;
constexpr unsigned WS_BYTES = 16u << 20;

DI void set_flag(char* ws, unsigned idx, unsigned tag) { __hip_atomic_store((GAS unsigned*)(ws + WS_FLAG + idx * 128), tag, __ATOMIC_RELAXED, __HIP_MEMORY_SCOPE_AGENT); }
DI void wait_flags(char* ws, unsigned idx, unsigned tag) {
  GAS unsigned* p = (GAS unsigned*)(ws + WS_FLAG + idx * 128);
#pragma unroll 1
  for (int spins = 0; spins < (1 << 18); ++spins) {
    const unsigned v = __hip_atomic_load(p, __ATOMIC_RELAXED, __HIP_MEMORY_SCOPE_AGENT);
    if (__all(v == tag)) break;
    __builtin_amdgcn_s_sleep(1);
  }
  asm volatile("" ::: "memory");
}
#define ST(n)
#define STEND()

constexpr int SLOT = 40960, XB = 16384, WB = 12288;
constexpr int G_RED = 122880;
constexpr int G_KVF = 122880 + 8192;
constexpr int G_OUT = 122880 + 16384;
constexpr int HCP = 4640, HCH = 4 * HCP;
constexpr int A_X = 5 * HCH, XCH = 4224;
constexpr int A_K = A_X, A_V = A_X + 21504;

__global__ void __launch_bounds__(512, 2)
fused_kernel(const u16* __restrict__ Aq, const u16* __restrict__ Ak, const u16* __restrict__ Av, const u16* __restrict__ Ask,
             const u16* __restrict__ Ah, const u16* __restrict__ AD, const u16* __restrict__ Bx, const u16* __restrict__ Bw,
             const u16* __restrict__ Bo, u16* __restrict__ out, char* __restrict__ ws) {
  __shared__ __attribute__((aligned(16))) char smem[163840];
  const int b = blockIdx.x, xcd = b & 7, role = (b >> 7), j = (b >> 3) & 15;
  const int tid = threadIdx.x, lane = tid & 63;
  const int wv = __builtin_amdgcn_readfirstlane(tid >> 6), lw = wv & 3;
  const unsigned L = (unsigned)(size_t)(LAS char*)smem;
  const int g = lane >> 4, r16 = lane & 15, q4 = r16 >> 2, p4 = lane & 3;
  const rsrc_t rws = mk(ws, WS_BYTES);
  GAS unsigned* pe = (GAS unsigned*)(ws + WS_EPOCH + (role ? xcd : 8) * 128);
  unsigned stampv = 0; (void)stampv;
  ST(0);

  if (role == 0) {
    const int rb = (xcd >> 1) * 4 + (j >> 2), cg = (xcd & 1) * 4 + (j & 3);
    const int a0 = (160 * cg) & ~63, dl = 160 * cg - a0;
    unsigned tag;
    if (wv < 4) {
      const int rot = ROT ? ((((j >> 2) + (j & 3)) & 3) * 5 + 1) >> 1 : 0;
      const i32x4 sx = make_srd(Bx, 2048 * 640 * 2), sw = make_srd(Bw, 640 * 1920 * 2);
      unsigned vx; { const int row = 8 * lw + (lane >> 3); vx = (rb * 128 + row) * 1280 + (((lane & 7) ^ ((row >> 1) & 7)) << 4); }
      unsigned vw[3], vw2[3];
#pragma unroll
      for (int u = 0; u < 3; u++) { const unsigned c = u * 64 + lane, row = (c * 171) >> 11, pos = c - row * 12; vw[u] = row * 3840 + ((pos ^ ((row >> 3) << 1)) << 4); vw2[u] = vw[u] ^ 16; }
      int sp = rot;
      auto issue = [&](int slot) {
        const unsigned lb = L + slot * SLOT;
#pragma unroll
        for (int u = 0; u < 4; u++) dma16(sx, vx, sp * 128 + u * 32 * 1280, lb + (lw + 4 * u) * 1024);
        const unsigned so = (sp * 64 + 16 * lw) * 3840 + 1280 + a0;
#pragma unroll
        for (int u = 0; u < 3; u++) dma16(sw, vw[u], so, lb + XB + (lw * 3 + u) * 1024);
#pragma unroll
        for (int u = 0; u < 3; u++) dma16(sw, vw2[u], so + 1280, lb + XB + WB + (lw * 3 + u) * 1024);
        sp = (sp == 9) ? 0 : sp + 1;
      };
      auto issue_q = [&](int pos) {
        const unsigned so = (sp * 64 + 16 * lw) * 3840 + a0;
#pragma unroll
        for (int u = 0; u < 3; u++) dma16(sw, vw[u], so, L + pos + (lw * 3 + u) * 1024);
        sp = (sp == 9) ? 0 : sp + 1;
      };
      issue(0); issue(1);
      tag = __hip_atomic_load(pe, __ATOMIC_RELAXED, __HIP_MEMORY_SCOPE_AGENT) + 1u;
      unsigned aX[2]; { const int row = lw * 32 + r16;
#pragma unroll
        for (int kk = 0; kk < 2; kk++) aX[kk] = L + row * 128 + (((4 * kk + g) ^ ((row >> 1) & 7)) << 4); }
      u32x4 xq[10][2][2];
      constexpr int QP[10] = {SLOT, SLOT + WB, SLOT + 2 * WB, SLOT + 3 * WB, SLOT + 4 * WB, SLOT + 5 * WB, 0, WB, 2 * WB, G_OUT};
#pragma unroll
      for (int s = 0; s < 10; s++) {
        if (s == 0) WAITV(11); else if (s < 9) WAITV(10); else WAITV(9);
        WAITL0(); BAR();
#pragma unroll
        for (int kk = 0; kk < 2; kk++)
#pragma unroll
          for (int m = 0; m < 2; m++) xq[s][kk][m] = ld128(aX[kk] + (s % 3) * SLOT, m * 2048);
        if (s < 8) issue((s + 2) % 3);
        else if (s == 8) { issue_q(QP[0]); issue_q(QP[1]); issue_q(QP[2]); }
        else { issue_q(QP[3]); issue_q(QP[4]); issue_q(QP[5]); }
      }
      WAITV(9); WAITL0(); BAR();
      issue_q(QP[6]); issue_q(QP[7]); issue_q(QP[8]); issue_q(QP[9]);
      ST(1);
      unsigned aQ[5];
#pragma unroll
      for (int n = 0; n < 5; n++) { const int off = (n < 4 ? dl + 40 * n + 8 * p4 : dl + 40 * p4 + 32) ^ ((g & 1) << 5); aQ[n] = L + (8 * g + q4) * 192 + off; }
      f32x4 qa[2][5];
#pragma unroll
      for (int m = 0; m < 2; m++)
#pragma unroll
        for (int n = 0; n < 5; n++) qa[m][n] = (f32x4){0, 0, 0, 0};
      {
        constexpr int PD = 4;
        u32x2 fw[PD + 1][2];
        auto rdw = [&](int it) { const int ks = it / 5, n = it % 5, pos = QP[ks >> 1] + (ks & 1) * 32 * 192;
          const unsigned base = aQ[n] + (pos & 0x38000); const int o = pos & 0x7fff;
          fw[it % (PD + 1)][0] = ldtr(base, o); fw[it % (PD + 1)][1] = ldtr(base, o + 768); };
#pragma unroll
        for (int it = 0; it < PD; it++) rdw(it);
#pragma unroll
        for (int it = 0; it < 100; it++) {
          const int ks = it / 5, n = it % 5;
          if (it + PD == 30) { ST(8); WAITV(0); ST(9); BAR(); ST(10); }
          if (it + PD < 100) rdw(it + PD);
          const u32x4 w = cat(fw[it % (PD + 1)][0], fw[it % (PD + 1)][1]);
          qa[0][n] = mfma16(w, xq[ks >> 1][ks & 1][0], qa[0][n]);
          qa[1][n] = mfma16(w, xq[ks >> 1][ks & 1][1], qa[1][n]);
          SCHED();
        }
      }
      ST(11);
      u32x2 pq[2][5];
#pragma unroll
      for (int m = 0; m < 2; m++)
#pragma unroll
        for (int n = 0; n < 5; n++) { pq[m][n][0] = pk(phi(qa[m][n][0]), phi(qa[m][n][1])); pq[m][n][1] = pk(phi(qa[m][n][2]), phi(qa[m][n][3])); }
      ST(2);
      WAITL0(); BAR();
      ST(3);
      float tl[2][4]; u32x2 oe[2][4];
#pragma unroll
      for (int h = 0; h < 4; h++) {
        const int hh = h & 1; const unsigned base = L + G_KVF + (h >> 1) * 2048, zero = L + G_KVF + 4096;
        const int col = (p4 == 0) ? 8 + 4 * hh + q4 : 4 * hh;
        const u32x4 e0 = cat(ld64(base + lane * 16, 8 * hh), ld64(g == h ? base + 1024 + (16 * hh + r16) * 16 + 8 : zero, 0));
        const u32x4 e1 = cat(ld64(base + 1024 + (16 * g + col) * 16, 0), ld64(g == h ? base + 1024 + (32 + 16 * hh + col) * 16 + 8 : zero, 0));
#pragma unroll
        for (int m = 0; m < 2; m++) {
          const u32x4 bq = cat(pq[m][h], pq[m][4]);
          const f32x4 n0 = mfma16(e0, bq, (f32x4){0, 0, 0, 0}), n1 = mfma16(e1, bq, (f32x4){0, 0, 0, 0});
          const float rc = __builtin_amdgcn_rcpf(n1[1] + 1e-6f);
          oe[m][h][0] = pk(n0[0] * rc, n0[1] * rc); oe[m][h][1] = pk(n0[2] * rc, n0[3] * rc); tl[m][h] = n1[0] * rc;
        }
      }
#pragma unroll
      for (int m = 0; m < 2; m++) {
        const unsigned so = WS_O + (rb * 8 + lw * 2 + m) * 20480 + (cg >> 1) * 5120;
        gst128c(rws, lane * 16, so + (cg & 1) * 2048, cat(oe[m][0], oe[m][1]));
        gst128c(rws, lane * 16, so + (cg & 1) * 2048 + 1024, cat(oe[m][2], oe[m][3]));
        gst64c(rws, lane * 8, so + 4096 + (cg & 1) * 512, (u32x2){pk(tl[m][0], tl[m][1]), pk(tl[m][2], tl[m][3])});
      }
      ST(12);
      WAITV(0);
      if (lane == 0) set_flag(ws, FL_O + (rb * 4 + lw) * 8 + cg, tag);
      ST(4);
    } else {
      tag = __hip_atomic_load(pe, __ATOMIC_RELAXED, __HIP_MEMORY_SCOPE_AGENT) + 1u;
      const int rg = lw >> 1, c = lw & 1;
      unsigned aA[2]; { const int row = rg * 64 + r16;
#pragma unroll
        for (int kk = 0; kk < 2; kk++) aA[kk] = L + row * 128 + (((4 * kk + g) ^ ((row >> 1) & 7)) << 4); }
      unsigned aB[5];
#pragma unroll
      for (int n = 0; n < 5; n++) { int sec, off; if (n < 4) { sec = n >> 1; off = dl + 80 * c + 40 * (n & 1) + 8 * p4; } else { sec = p4 >> 1; off = dl + 80 * c + 40 * (p4 & 1) + 32; }
        off ^= ((g & 1) << 5) ^ (sec << 4); aB[n] = L + XB + sec * WB + (8 * g + q4) * 192 + off; }
      if (lane == 0) st128(L + G_KVF + 4096, 0, (u32x4){0, 0, 0, 0});
      f32x4 acc[4][5];
      u32x4 fa[2][4]; u32x2 fb[2][5][2];
#pragma unroll
      for (int m = 0; m < 4; m++) { fa[1][m] = (u32x4){0, 0, 0, 0};
#pragma unroll
        for (int n = 0; n < 5; n++) acc[m][n] = (f32x4){0, 0, 0, 0}; }
#pragma unroll
      for (int n = 0; n < 5; n++) { fb[1][n][0] = (u32x2){0, 0}; fb[1][n][1] = (u32x2){0, 0}; }
      int sl = 0, d = 0;
      auto half = [&](int rbuf, int kk) {
#pragma unroll
        for (int t = 0; t < 20; t++) {
          const int n = t >> 2, m = t & 3;
          acc[m][n] = mfma16(fa[1 - rbuf][m], cat(fb[1 - rbuf][n][0], fb[1 - rbuf][n][1]), acc[m][n]);
          SCHED();
          if (t < 4) fa[rbuf][t] = ld128(aA[kk], t * 2048);
          else if (t < 14) { const int n2 = (t - 4) >> 1, h = (t - 4) & 1; fb[rbuf][n2][h] = ldtr(aB[n2], kk * 32 * 192 + h * 768); }
          else if (kk == 0) { if (t == 14) { d = (sl == 2) ? -2 * SLOT : SLOT; sl = (sl == 2) ? 0 : sl + 1; } if (t == 15) { aA[0] += d; asm volatile("" : "+v"(aA[0])); } }
          else { if (t == 14) { aA[1] += d; aB[0] += d; asm volatile("" : "+v"(aA[1]), "+v"(aB[0])); } if (t == 15) { aB[1] += d; aB[2] += d; asm volatile("" : "+v"(aB[1]), "+v"(aB[2])); } if (t == 16) { aB[3] += d; aB[4] += d; asm volatile("" : "+v"(aB[3]), "+v"(aB[4])); } }
          SCHED();
        }
      };
      asm volatile("" : "+v"(tag));
      WAITL0();
#pragma unroll 1
      for (int s = 0; s < 10; s++) { SCHED(); WAITL0(); BAR(); SCHED(); half(0, 0); half(1, 1); }
      WAITL0(); BAR(); SCHED();
#pragma unroll
      for (int n = 0; n < 5; n++)
#pragma unroll
        for (int m = 0; m < 4; m++) acc[m][n] = mfma16(fa[1][m], cat(fb[1][n][0], fb[1][n][1]), acc[m][n]);
      ST(1);
      f32x4 R[7];
#pragma unroll
      for (int i = 0; i < 7; i++) R[i] = (f32x4){0, 0, 0, 0};
#pragma unroll
      for (int ks = 0; ks < 2; ks++) {
        u32x4 T[6];
#pragma unroll
        for (int hf = 0; hf < 2; hf++) { const int m = 2 * ks + hf;
#pragma unroll
          for (int e = 0; e < 2; e++) {
            T[0][2 * hf + e] = pk(phi(acc[m][0][2 * e]), phi(acc[m][0][2 * e + 1]));
            T[1][2 * hf + e] = pk(phi(acc[m][1][2 * e]), phi(acc[m][1][2 * e + 1]));
            T[2][2 * hf + e] = pk(acc[m][2][2 * e], acc[m][2][2 * e + 1]);
            T[3][2 * hf + e] = pk(acc[m][3][2 * e], acc[m][3][2 * e + 1]);
            T[4][2 * hf + e] = pk(phi(acc[m][4][2 * e]), phi(acc[m][4][2 * e + 1]));
            T[5][2 * hf + e] = r16 < 8 ? 0x3f803f80u : pk(acc[m][4][2 * e], acc[m][4][2 * e + 1]);
          } }
        R[0] = mfma16(T[0], T[2], R[0]); R[1] = mfma16(T[1], T[3], R[1]);
        R[2] = mfma16(T[0], T[5], R[2]); R[3] = mfma16(T[1], T[5], R[3]);
        R[4] = mfma16(T[4], T[2], R[4]); R[5] = mfma16(T[4], T[3], R[5]); R[6] = mfma16(T[4], T[5], R[6]);
      }
      f32x4 M23, M456;
#pragma unroll
      for (int i = 0; i < 4; i++) {
        M23[i] = (q4 & 1) ? R[3][i] : R[2][i];
        const float r45 = (g == 0) ? R[4][i] : R[5][i];
        M456[i] = __uint_as_float(__builtin_amdgcn_permlane32_swap(__float_as_uint(r45), __float_as_uint(R[6][i]), false, false)[0]);
      }
      f32x4 k0 = rg ? M23 : R[0], k1 = rg ? M456 : R[1];
      { const f32x4 s0 = rg ? R[0] : M23, s1 = rg ? R[1] : M456;
        st128(L + G_RED + lw * 2048 + lane * 16, 0, __builtin_bit_cast(u32x4, s0)); st128(L + G_RED + lw * 2048 + lane * 16, 1024, __builtin_bit_cast(u32x4, s1)); }
      ST(8); WAITL0(); BAR(); ST(9);
      k0 += __builtin_bit_cast(f32x4, ld128(L + G_RED + (lw ^ 2) * 2048 + lane * 16, 0));
      k1 += __builtin_bit_cast(f32x4, ld128(L + G_RED + (lw ^ 2) * 2048 + lane * 16, 1024));
      const int irow = c * 2 + rg;
      gst128c(rws, lane * 16, WS_PART + (cg * 16 + rb) * 4096 + irow * 1024, (u32x4){pk(k0[0], k0[1]), pk(k0[2], k0[3]), pk(k1[0], k1[1]), pk(k1[2], k1[3])});
      ST(10);
      WAITV(0);
      if (lane == 0) set_flag(ws, FL_P + (cg * 16 + rb) * 4 + irow, tag);
      ST(2);
      wait_flags(ws, FL_P + (cg * 16 + r16) * 4 + irow, tag);
      ST(3);
      u32x4 pv[4][4];
#pragma unroll
      for (int blk = 0; blk < 4; blk++)
#pragma unroll
        for (int rp = 0; rp < 4; rp++) pv[blk][rp] = gld128c(rws, g * 4096 + r16 * 16 + blk * 256, WS_PART + (cg * 16 + rp * 4) * 4096 + irow * 1024);
      u32x4 sel[2];
#pragma unroll
      for (int o = 0; o < 2; o++)
#pragma unroll
        for (int i = 0; i < 4; i++) sel[o][i] = ((r16 >> 1) == 4 * o + i) ? ((r16 & 1) ? 0x3f800000u : 0x00003f80u) : 0u;
#pragma unroll
      for (int bp = 0; bp < 2; bp++) {
        f32x4 sum = {0, 0, 0, 0};
#pragma unroll
        for (int o = 0; o < 2; o++)
#pragma unroll
          for (int rp = 0; rp < 4; rp++) sum = mfma16(sel[o], pv[2 * bp + o][rp], sum);
        st64(L + G_KVF + irow * 1024 + ((2 * bp + (g >> 1)) * 16 + r16) * 16 + (g & 1) * 8, 0, (u32x2){pk(sum[0], sum[1]), pk(sum[2], sum[3])});
      }
      ST(11); WAITL0(); BAR();
      ST(4);
      const i32x4 so_ = make_srd(Bo, 640 * 640 * 2);
      unsigned vo[3];
#pragma unroll
      for (int u = 0; u < 3; u++) { const unsigned cc = u * 64 + lane, row = (cc * 171) >> 11, pos = cc - row * 12; vo[u] = row * 1280 + ((pos ^ (((row >> 2) & 1) << 1)) << 4); }
#pragma unroll 1
      for (int i = 0; i < 10; i++) { const int gr = i * 4 + lw;
#pragma unroll
        for (int u = 0; u < 3; u++) dma16(so_, vo[u], gr * 16 * 1280 + a0, L + (gr * 3 + u) * 1024); }
      ST(12); WAITV(0); ST(13);
    }
    wait_flags(ws, FL_O + (rb * 4 + (wv >> 1)) * 8 + (lane & 7), tag);
    ST(5);
    u32x4 of[20];
#pragma unroll
    for (int p = 0; p < 4; p++) {
      const unsigned so = WS_O + (rb * 8 + wv) * 20480 + p * 5120;
#pragma unroll
      for (int f = 0; f < 4; f++) of[5 * p + f] = gld128c(rws, lane * 16 + f * 1024, so);
      of[5 * p + 4] = cat(gld64c(rws, lane * 8, so + 4096), gld64c(rws, lane * 8 + 512, so + 4096));
    }
    if (b == 0 && tid == 0) __hip_atomic_store(pe, tag, __ATOMIC_RELAXED, __HIP_MEMORY_SCOPE_AGENT);
    BAR();
    ST(14);
    unsigned aW[2][2], aT[2];
#pragma unroll
    for (int e = 0; e < 2; e++) {
#pragma unroll
      for (int par = 0; par < 2; par++) aW[par][e] = L + (4 * g + q4) * 192 + ((dl + 32 * e + 8 * p4) ^ (((g + par) & 1) << 5));
      aT[e] = L + (20 * q4 + 16 + g) * 192 + ((dl + 32 * e + 8 * p4) ^ ((q4 & 1) << 5));
    }
    f32x4 oa[5];
#pragma unroll
    for (int n = 0; n < 5; n++) oa[n] = (f32x4){0, 0, 0, 0};
#pragma unroll
    for (int p = 0; p < 4; p++) {
#pragma unroll
      for (int f = 0; f < 5; f++)
#pragma unroll
        for (int n = 0; n < 5; n++) {
          const int co = (n >> 1) * 64;
          u32x4 w;
          if (f < 4) w = cat(ldtr(aW[0][n & 1], f * 40 * 192 + co), ldtr(aW[1][n & 1], (f * 40 + 20) * 192 + co));
          else w = cat(ldtr(aT[n & 1], co), ldtr(aT[n & 1], 80 * 192 + co));
          oa[n] = mfma16(w, of[5 * p + f], oa[n]);
        }
#pragma unroll
      for (int e = 0; e < 2; e++) { aW[0][e] += 160 * 192; aW[1][e] += 160 * 192; aT[e] += 160 * 192; }
    }
    ST(6);
    const unsigned so = L + G_OUT + wv * 2560;
#pragma unroll
    for (int n = 0; n < 5; n++) st64(so + r16 * 160 + g * 8, n * 32, (u32x2){pk(oa[n][0], oa[n][1]), pk(oa[n][2], oa[n][3])});
    const rsrc_t ro = mk(out, 2048 * 1280 * 2);
#pragma unroll
    for (int u = 0; u < 3; u++) { const unsigned cc = u * 64 + lane, row = (cc * 205) >> 11;
      if (u < 2 || lane < 32) __builtin_amdgcn_raw_buffer_store_b128(ld128(so + cc * 16, 0), ro, row * 2400 + cc * 16, (rb * 128 + wv * 16) * 2560 + 1280 + cg * 160, 17); }
    ST(7); STEND();
  } else {
    const int xg = xcd;
    const i32x4 sk_ = make_srd(Ak, 2048 * 1280), sv_ = make_srd(Av, 2048 * 1280);
    unsigned vr[3];
#pragma unroll
    for (int u = 0; u < 3; u++) { const unsigned c = tid + 512 * u, row = (c * 6554) >> 16; vr[u] = (128 * j + row) * 1280 + (c - row * 10) * 16 + xg * 160; }
#pragma unroll
    for (int u = 0; u < 3; u++) {
      if (u < 2 || wv < 5) dma16(sk_, vr[u] - 4 * 1280, 0, L + A_K + (u * 8 + wv) * 1024);
      if (u < 2 || wv < 4) dma16(sv_, vr[u], 0, L + A_V + (u * 8 + wv) * 1024);
    }
    const unsigned tag = __hip_atomic_load(pe, __ATOMIC_RELAXED, __HIP_MEMORY_SCOPE_AGENT) + 1u;
    u32x2 wk[3];
#pragma unroll
    for (int u = 0; u < 3; u++) { const int tk = (wv + 8 * u) % 5; wk[u] = *(const GAS u32x2*)(Ask + (80 * xg + 16 * tk + r16) * 4); }
    const rsrc_t rh = mk(Ah, 640 * 4096);
    u32x4 hv[3]; u32x2 hp[3];
#pragma unroll
    for (int u = 0; u < 3; u++) if (u < 2 || wv < 4) { const unsigned c = tid + 512 * u;
      hv[u] = gld128(rh, c * 16, (80 * xg + 5 * j) * 4096);
      hp[u] = __builtin_amdgcn_raw_buffer_load_b64(rh, (c & 255) ? (80 * xg + 5 * j) * 4096 + c * 16 - 8 : 0xfffffff0u, 0, 0); }
    if (tid < 480) { const unsigned ar = (tid * 2731) >> 16; st128(L + ar * HCP + (tid - ar * 24) * 16, 0, (u32x4){0, 0, 0, 0}); }
    WAITV(0); BAR();
    ST(1);
#pragma unroll
    for (int u = 0; u < 3; u++) if (u < 2 || wv < 4) {
      const int tk = wv + 8 * u, sq = (tk * 13) >> 6, ct = tk - 5 * sq, un = 4 * sq + g;
      const unsigned ad = L + (8 * un + q4) * 160 + 32 * ct + 8 * p4;
      const u32x2 k0 = ldtr(ad, A_K), k1 = ldtr(ad, A_K + 640), k2 = ldtr(ad, A_K + 1280), v0 = ldtr(ad + A_V, 0), v1 = ldtr(ad + A_V, 640);
      const float kf[11] = {bhi(k0[0]), blo(k0[1]), bhi(k0[1]), blo(k1[0]), bhi(k1[0]), blo(k1[1]), bhi(k1[1]), blo(k2[0]), bhi(k2[0]), blo(k2[1]), bhi(k2[1])};
      const float vf[8] = {blo(v0[0]), bhi(v0[0]), blo(v0[1]), bhi(v0[1]), blo(v1[0]), bhi(v1[0]), blo(v1[1]), bhi(v1[1])};
      const float w0 = blo(wk[u][0]), w1 = bhi(wk[u][0]), w2 = blo(wk[u][1]), w3 = bhi(wk[u][1]);
      float o[8];
#pragma unroll
      for (int i = 0; i < 8; i++) o[i] = (kf[i] * w3 + kf[i + 1] * w2 + kf[i + 2] * w1 + kf[i + 3] * w0) * vf[i];
      gst128c(rws, (255 - 16 * j - un) * 1280 + (16 * ct + r16) * 16, WS_KVT + xg * 327680, (u32x4){pk(o[7], o[6]), pk(o[5], o[4]), pk(o[3], o[2]), pk(o[1], o[0])});
    }
    ST(8);
#pragma unroll
    for (int u = 0; u < 3; u++) if (u < 2 || wv < 4) { const unsigned c = tid + 512 * u;
      const unsigned ad = L + (c >> 8) * HCH + 384 + (c & 255) * 16;
      const unsigned e0 = __builtin_amdgcn_alignbyte(hp[u][1], hp[u][0], 2), e1 = __builtin_amdgcn_alignbyte(hv[u][0], hp[u][1], 2), e2 = __builtin_amdgcn_alignbyte(hv[u][1], hv[u][0], 2),
                     e3 = __builtin_amdgcn_alignbyte(hv[u][2], hv[u][1], 2), e4 = __builtin_amdgcn_alignbyte(hv[u][3], hv[u][2], 2);
      st128(ad, 0, hv[u]); st128(ad, HCP, (u32x4){e1, e2, e3, e4}); st128(ad, 2 * HCP, (u32x4){hp[u][1], hv[u][0], hv[u][1], hv[u][2]}); st128(ad, 3 * HCP, (u32x4){e0, e1, e2, e3}); }
    ST(9);
    WAITV(0); WAITL0(); BAR();
    if (tid == 0) set_flag(ws, FL_K + xg * 16 + j, tag);
    ST(2);
    if (tid < 40) st128(L + A_X + (tid >> 3) * XCH + 4096 + (tid & 7) * 16, 0, (u32x4){0, 0, 0, 0});
    const int chl = wv < 4 ? wv : 4;
    const float Dc = __uint_as_float((unsigned)AD[80 * xg + 5 * j + chl] << 16);
    if (wv == 0) wait_flags(ws, FL_K + xg * 16 + r16, tag);
    BAR();
    ST(3);
    { u32x4 xv[3]; unsigned xa[3];
#pragma unroll
      for (int u = 0; u < 3; u++) if (u < 2 || wv < 4) { const unsigned c = tid + 512 * u, blk = (c * 13108) >> 16, ch = c - 5 * blk;
        xv[u] = gld128c(rws, blk * 1280 + ch * 16, WS_KVT + xg * 327680 + j * 80); xa[u] = L + A_X + ch * XCH + blk * 16; }
      if (j == 0 && tid == 0) __hip_atomic_store(pe, tag, __ATOMIC_RELAXED, __HIP_MEMORY_SCOPE_AGENT);
#pragma unroll
      for (int u = 0; u < 3; u++) if (u < 2 || wv < 4) st128(xa[u], 0, xv[u]); }
    const rsrc_t rq = mk(Aq, 2048 * 1280);
    u32x4 qv[3];
#pragma unroll
    for (int u = 0; u < 3; u++) if (u < 2 || wv < 4) qv[u] = gld128(rq, vr[u], 0);
    WAITL0(); BAR();
    ST(4);
    unsigned aH = L + chl * HCH + (3 - p4) * HCP + (64 * q4 + 8 * g) * 2;
    unsigned aXr = L + A_X + chl * XCH + (1852 - 1792 - 4 * r16 + 8 * g) * 2;
    const unsigned aE = L + A_X + chl * XCH + (2044 - 1792 - 64 * g - 4 * r16) * 2;
    const unsigned yo = WS_YT + (80 * xg + 5 * j + chl) * 4096;
    auto fin = [&](f32x4 a, u32x2 xr, unsigned off) {
      gst64c(rws, lane * 8 + off, yo, (u32x2){pk(a[0] + Dc * bhi(xr[1]), a[1] + Dc * blo(xr[1])), pk(a[2] + Dc * bhi(xr[0]), a[3] + Dc * blo(xr[0]))}); };
    if (wv < 4) {
      f32x4 acc[8];
      u32x4 F[2][8]; u32x2 G[2][8][2];
#pragma unroll
      for (int t = 0; t < 8; t++) { acc[t] = (f32x4){0, 0, 0, 0}; F[0][t] = ld128(aH, 512 * t); G[0][t][0] = ld64(aXr, 512 * (7 - t)); G[0][t][1] = ld64(aXr, 512 * (7 - t) + 8); }
      auto step = [&](int cur, int off) {
        int cnt = 0;
#pragma unroll
        for (int t = 0; t < 8; t++)
#pragma unroll
          for (int p = 0; p <= t; p++) {
            const int tt = t, pp = p;
            acc[tt] = mfma16(F[cur][pp], cat(G[cur][tt - pp][0], G[cur][tt - pp][1]), acc[tt]);
            SCHED();
            if (cnt < 24) { const int i = cnt / 3, w = cnt % 3;
              if (w == 0) F[1 - cur][i] = ld128(aH, 512 * i + off); else G[1 - cur][i][w - 1] = ld64(aXr, 512 * (7 - i) + off + 8 * (w - 1)); }
            if (cnt == 24 && cur == 1) { aH += 128; aXr += 128; asm volatile("" : "+v"(aH), "+v"(aXr)); }
            cnt++;
            SCHED();
          }
      };
#pragma unroll 1
      for (int r = 0; r < 4; r++) { step(0, 64); step(1, 128); }
      ST(10);
#pragma unroll
      for (int t = 0; t < 8; t++) fin(acc[t], ld64(aE, 512 * (7 - t)), 512 * t);
    } else {
#pragma unroll 1
      for (int z = 0; z < 2; z++) {
        const int t = z ? lw : 7 - lw;
        unsigned h2 = aH, x2 = aXr + 512 * (7 - t);
        f32x4 a0_ = {0, 0, 0, 0}, a1_ = {0, 0, 0, 0};
#pragma unroll 1
        for (int m = 0; m <= t; m++) {
#pragma unroll
          for (int r = 0; r < 8; r += 2) {
            a0_ = mfma16(ld128(h2, 64 * r), cat(ld64(x2, 64 * r), ld64(x2, 64 * r + 8)), a0_);
            a1_ = mfma16(ld128(h2, 64 * r + 64), cat(ld64(x2, 64 * r + 64), ld64(x2, 64 * r + 72)), a1_);
          }
          h2 += 512; x2 += 512;
        }
        fin(a0_ + a1_, ld64(aE + 512 * (7 - t), 0), 512 * t);
      }
    }
    ST(5);
    WAITV(0); WAITL0(); BAR();
    if (tid == 0) set_flag(ws, FL_Y + xg * 16 + j, tag);
    ST(11);
    if (wv == 0) wait_flags(ws, FL_Y + xg * 16 + r16, tag);
    BAR();
    ST(6);
    { u32x4 yv[3];
#pragma unroll
      for (int u = 0; u < 3; u++) if (u < 2 || wv < 4) yv[u] = gld128c(rws, ((tid >> 4) + 32 * u) * 4096 + r16 * 16, WS_YT + 80 * xg * 4096 + j * 256);
      const unsigned ad = L + r16 * (8 * 160 + 16) + (tid >> 4) * 2;
#pragma unroll
      for (int u = 0; u < 3; u++) if (u < 2 || wv < 4) {
#pragma unroll
        for (int e = 0; e < 4; e++) { st16(ad, 64 * u + 320 * e, (u16)yv[u][e]); st16(ad, 64 * u + 320 * e + 160, (u16)(yv[u][e] >> 16)); } } }
    WAITL0(); BAR();
    ST(12);
    const rsrc_t ro = mk(out, 2048 * 1280 * 2);
#pragma unroll
    for (int u = 0; u < 3; u++) if (u < 2 || wv < 4) { const unsigned c = tid + 512 * u, row = (c * 6554) >> 16;
      const u32x4 y = ld128(L + c * 16 + (row >> 3) * 16, 0); u32x4 r;
#pragma unroll
      for (int e = 0; e < 4; e++) r[e] = pk(blo(y[e]) * blo(qv[u][e]), bhi(y[e]) * bhi(qv[u][e]));
      __builtin_amdgcn_raw_buffer_store_b128(r, ro, vr[u] + (128 * j + row) * 1280, 0, 17); }
    ST(7); STEND();
  }
}

extern "C" void kernel_launch(void* const* d_in, const int* in_sizes, int n_in, void* d_out, int out_size, void* d_ws, size_t ws_size, hipStream_t stream) {
  fused_kernel<<<256, 512, 0, stream>>>((const u16*)d_in[0], (const u16*)d_in[1], (const u16*)d_in[2], (const u16*)d_in[3], (const u16*)d_in[4], (const u16*)d_in[5],
                                        (const u16*)d_in[6], (const u16*)d_in[7], (const u16*)d_in[8], (u16*)d_out, (char*)d_ws);
}
```

```cpp
#include <hip/hip_runtime.h>
#include <hip/hip_bf16.h>

typedef unsigned short u16;
typedef unsigned long long u64;
typedef short s16x4 __attribute__((ext_vector_type(4)));
typedef short s16x8 __attribute__((ext_vector_type(8)));
typedef __bf16 bf16x2 __attribute__((ext_vector_type(2)));
typedef __bf16 bf16x8 __attribute__((ext_vector_type(8)));
typedef float f32x2 __attribute__((ext_vector_type(2)));
typedef float f32x4 __attribute__((ext_vector_type(4)));
typedef unsigned u32x2 __attribute__((ext_vector_type(2)));
typedef unsigned u32x4 __attribute__((ext_vector_type(4)));
typedef int i32x4 __attribute__((ext_vector_type(4)));
#define LAS __attribute__((address_space(3)))
#define GAS __attribute__((address_space(1)))
#define DI __device__ __forceinline__
#define WAITV(n) asm volatile("s_waitcnt vmcnt(" #n ")" ::: "memory")
#define WAITL0() __builtin_amdgcn_s_waitcnt(0xC07F)
#define BAR() do { asm volatile("" ::: "memory"); __builtin_amdgcn_s_barrier(); asm volatile("" ::: "memory"); } while (0)
#define SCHED() __builtin_amdgcn_sched_barrier(0)
#define ROT 1
#define STAMP 0

DI f32x4 mfma16(u32x4 a, u32x4 b, f32x4 c) { return __builtin_amdgcn_mfma_f32_16x16x32_bf16(__builtin_bit_cast(bf16x8, a), __builtin_bit_cast(bf16x8, b), c, 0, 0, 0); }
DI u32x4 cat(u32x2 a, u32x2 b) { return __builtin_shufflevector(a, b, 0, 1, 2, 3); }
DI u32x4 ld128(unsigned a, int off) { return *(const LAS u32x4*)((LAS char*)(size_t)a + off); }
DI u32x2 ld64(unsigned a, int off) { return *(const LAS u32x2*)((LAS char*)(size_t)a + off); }
DI u32x2 ldtr(unsigned a, int off) { return __builtin_bit_cast(u32x2, __builtin_amdgcn_ds_read_tr16_b64_v4i16((LAS s16x4*)((LAS char*)(size_t)a + off))); }
DI void st128(unsigned a, int off, u32x4 v) { *(LAS u32x4*)((LAS char*)(size_t)a + off) = v; }
DI void st64(unsigned a, int off, u32x2 v) { *(LAS u32x2*)((LAS char*)(size_t)a + off) = v; }
DI void st16(unsigned a, int off, u16 v) { *(LAS u16*)((LAS char*)(size_t)a + off) = v; }
DI unsigned pk(float lo, float hi) { f32x2 v = {lo, hi}; return __builtin_bit_cast(unsigned, __builtin_convertvector(v, bf16x2)); }
DI float blo(unsigned u) { return __uint_as_float(u << 16); }
DI float bhi(unsigned u) { return __uint_as_float(u & 0xffff0000u); }
DI float phi(float t) { return __builtin_fmaxf(t, 0.f) + __builtin_amdgcn_exp2f(__builtin_fminf(t, 0.f) * 1.44269504088896341f) + 1e-6f; }
DI i32x4 make_srd(const void* p, unsigned bytes) { u64 a = (u64)p; i32x4 r; r[0] = (int)(unsigned)a; r[1] = (int)(unsigned)(a >> 32) & 0xffff; r[2] = (int)bytes; r[3] = 0x00020000; return r; }
DI void dma16(i32x4 srd, unsigned voff, unsigned soff, unsigned m0v) {
  asm volatile("s_mov_b32 m0, %3\n\ts_nop 0\n\tbuffer_load_dwordx4 %0, %1, %2 offen lds" ::"v"(voff), "s"(srd), "s"(soff), "s"(m0v) : "memory");
}
typedef __amdgpu_buffer_rsrc_t rsrc_t;
DI rsrc_t mk(const void* p, unsigned bytes) { return __builtin_amdgcn_make_buffer_rsrc((void*)p, 0, bytes, 0x00020000); }
constexpr int SC1 = 16;
DI u32x4 gld128(rsrc_t r, unsigned v, unsigned s) { return __builtin_amdgcn_raw_buffer_load_b128(r, v, s, 0); }
DI u32x4 gld128c(rsrc_t r, unsigned v, unsigned s) { return __builtin_amdgcn_raw_buffer_load_b128(r, v, s, SC1); }
DI u32x2 gld64c(rsrc_t r, unsigned v, unsigned s) { return __builtin_amdgcn_raw_buffer_load_b64(r, v, s, SC1); }
DI void gst128c(rsrc_t r, unsigned v, unsigned s, u32x4 d) { __builtin_amdgcn_raw_buffer_store_b128(d, r, v, s, SC1); }
DI void gst64c(rsrc_t r, unsigned v, unsigned s, u32x2 d) { __builtin_amdgcn_raw_buffer_store_b64(d, r, v, s, SC1); }

constexpr unsigned WS_EPOCH = 0;
constexpr unsigned WS_FLAG = 4096;
constexpr unsigned FL_P = 0;
constexpr unsigned FL_O = 512;
constexpr unsigned FL_K = 1024;
constexpr unsigned FL_Y = 1152;
constexpr unsigned WS_PART = 1u << 18;
constexpr unsigned WS_O = 1u << 20;
constexpr unsigned WS_KVT = 4u << 20;
constexpr unsigned WS_YT = 8u << 20;
constexpr unsigned WS_DBG = 12u << 20;
constexpr unsigned WS_BYTES = 16u << 20;

DI void set_flag(char* ws, unsigned idx, unsigned tag) { __hip_atomic_store((GAS unsigned*)(ws + WS_FLAG + idx * 128), tag, __ATOMIC_RELAXED, __HIP_MEMORY_SCOPE_AGENT); }
DI void wait_flags(char* ws, unsigned idx, unsigned tag) {
  GAS unsigned* p = (GAS unsigned*)(ws + WS_FLAG + idx * 128);
#pragma unroll 1
  for (int spins = 0; spins < (1 << 18); ++spins) {
    const unsigned v = __hip_atomic_load(p, __ATOMIC_RELAXED, __HIP_MEMORY_SCOPE_AGENT);
    if (__all(v == tag)) break;
    __builtin_amdgcn_s_sleep(1);
  }
  asm volatile("" ::: "memory");
}
#define ST(n)
#define STEND()

constexpr int SLOT = 40960, XB = 16384, WB = 12288;
constexpr int G_RED = 122880;
constexpr int G_KVF = 122880 + 8192;
constexpr int G_OUT = 122880 + 16384;
constexpr int HCP = 4640, HCH = 4 * HCP;
constexpr int A_X = 5 * HCH, XCH = 4224;
constexpr int A_K = A_X, A_V = A_X + 21504;

__global__ void __launch_bounds__(512, 2)
fused_kernel(const u16* __restrict__ Aq, const u16* __restrict__ Ak, const u16* __restrict__ Av, const u16* __restrict__ Ask,
             const u16* __restrict__ Ah, const u16* __restrict__ AD, const u16* __restrict__ Bx, const u16* __restrict__ Bw,
             const u16* __restrict__ Bo, u16* __restrict__ out, char* __restrict__ ws) {
  __shared__ __attribute__((aligned(16))) char smem[163840];
  const int b = blockIdx.x, xcd = b & 7, role = (b >> 7), j = (b >> 3) & 15;
  const int tid = threadIdx.x, lane = tid & 63;
  const int wv = __builtin_amdgcn_readfirstlane(tid >> 6), lw = wv & 3;
  const unsigned L = (unsigned)(size_t)(LAS char*)smem;
  const int g = lane >> 4, r16 = lane & 15, q4 = r16 >> 2, p4 = lane & 3;
  const rsrc_t rws = mk(ws, WS_BYTES);
  GAS unsigned* pe = (GAS unsigned*)(ws + WS_EPOCH + (role ? xcd : 8) * 128);
  unsigned stampv = 0; (void)stampv;
  ST(0);

  if (role == 0) {
    const int rb = (xcd >> 1) * 4 + (j >> 2), cg = (xcd & 1) * 4 + (j & 3);
    const int a0 = (160 * cg) & ~63, dl = 160 * cg - a0;
    unsigned tag;
    if (wv < 4) {
      const int rot = ROT ? ((((j >> 2) + (j & 3)) & 3) * 5 + 1) >> 1 : 0;
      const i32x4 sx = make_srd(Bx, 2048 * 640 * 2), sw = make_srd(Bw, 640 * 1920 * 2);
      unsigned vx; { const int row = 8 * lw + (lane >> 3); vx = (rb * 128 + row) * 1280 + (((lane & 7) ^ ((row >> 1) & 7)) << 4); }
      unsigned vw[3], vw2[3];
#pragma unroll
      for (int u = 0; u < 3; u++) { const unsigned c = u * 64 + lane, row = (c * 171) >> 11, pos = c - row * 12; vw[u] = row * 3840 + ((pos ^ ((row >> 3) << 1)) << 4); vw2[u] = vw[u] ^ 16; }
      int sp = rot;
      auto issue = [&](int slot) {
        const unsigned lb = L + slot * SLOT;
#pragma unroll
        for (int u = 0; u < 4; u++) dma16(sx, vx, sp * 128 + u * 32 * 1280, lb + (lw + 4 * u) * 1024);
        const unsigned so = (sp * 64 + 16 * lw) * 3840 + 1280 + a0;
#pragma unroll
        for (int u = 0; u < 3; u++) dma16(sw, vw[u], so, lb + XB + (lw * 3 + u) * 1024);
#pragma unroll
        for (int u = 0; u < 3; u++) dma16(sw, vw2[u], so + 1280, lb + XB + WB + (lw * 3 + u) * 1024);
        sp = (sp == 9) ? 0 : sp + 1;
      };
      auto issue_q = [&](int pos) {
        const unsigned so = (sp * 64 + 16 * lw) * 3840 + a0;
#pragma unroll
        for (int u = 0; u < 3; u++) dma16(sw, vw[u], so, L + pos + (lw * 3 + u) * 1024);
        sp = (sp == 9) ? 0 : sp + 1;
      };
      issue(0); issue(1);
      tag = __hip_atomic_load(pe, __ATOMIC_RELAXED, __HIP_MEMORY_SCOPE_AGENT) + 1u;
      unsigned aX[2]; { const int row = lw * 32 + r16;
#pragma unroll
        for (int kk = 0; kk < 2; kk++) aX[kk] = L + row * 128 + (((4 * kk + g) ^ ((row >> 1) & 7)) << 4); }
      u32x4 xq[10][2][2];
      constexpr int QP[10] = {SLOT, SLOT + WB, SLOT + 2 * WB, SLOT + 3 * WB, SLOT + 4 * WB, SLOT + 5 * WB, 0, WB, 2 * WB, G_OUT};
#pragma unroll
      for (int s = 0; s < 10; s++) {
        if (s == 0) WAITV(11); else if (s < 9) WAITV(10); else WAITV(9);
        WAITL0(); BAR();
#pragma unroll
        for (int kk = 0; kk < 2; kk++)
#pragma unroll
          for (int m = 0; m < 2; m++) xq[s][kk][m] = ld128(aX[kk] + (s % 3) * SLOT, m * 2048);
        if (s < 8) issue((s + 2) % 3);
        else if (s == 8) { issue_q(QP[0]); issue_q(QP[1]); issue_q(QP[2]); }
        else { issue_q(QP[3]); issue_q(QP[4]); issue_q(QP[5]); }
      }
      WAITV(9); WAITL0(); BAR();
      issue_q(QP[6]); issue_q(QP[7]); issue_q(QP[8]); issue_q(QP[9]);
      ST(1);
      unsigned aQ[5];
#pragma unroll
      for (int n = 0; n < 5; n++) { const int off = (n < 4 ? dl + 40 * n + 8 * p4 : dl + 40 * p4 + 32) ^ ((g & 1) << 5); aQ[n] = L + (8 * g + q4) * 192 + off; }
      f32x4 qa[2][5];
#pragma unroll
      for (int m = 0; m < 2; m++)
#pragma unroll
        for (int n = 0; n < 5; n++) qa[m][n] = (f32x4){0, 0, 0, 0};
      {
        constexpr int PD = 4;
        u32x2 fw[PD + 1][2];
        auto rdw = [&](int it) { const int ks = it / 5, n = it % 5, pos = QP[ks >> 1] + (ks & 1) * 32 * 192;
          const unsigned base = aQ[n] + (pos & 0x38000); const int o = pos & 0x7fff;
          fw[it % (PD + 1)][0] = ldtr(base, o); fw[it % (PD + 1)][1] = ldtr(base, o + 768); };
#pragma unroll
        for (int it = 0; it < PD; it++) rdw(it);
#pragma unroll
        for (int it = 0; it < 100; it++) {
          const int ks = it / 5, n = it % 5;
          if (it + PD == 30) { ST(8); WAITV(0); ST(9); BAR(); ST(10); }
          if (it + PD < 100) rdw(it + PD);
          const u32x4 w = cat(fw[it % (PD + 1)][0], fw[it % (PD + 1)][1]);
          qa[0][n] = mfma16(w, xq[ks >> 1][ks & 1][0], qa[0][n]);
          qa[1][n] = mfma16(w, xq[ks >> 1][ks & 1][1], qa[1][n]);
          SCHED();
        }
      }
      ST(11);
      u32x2 pq[2][5];
#pragma unroll
      for (int m = 0; m < 2; m++)
#pragma unroll
        for (int n = 0; n < 5; n++) { pq[m][n][0] = pk(phi(qa[m][n][0]), phi(qa[m][n][1])); pq[m][n][1] = pk(phi(qa[m][n][2]), phi(qa[m][n][3])); }
      ST(2);
      WAITL0(); BAR();
      ST(3);
      float tl[2][4]; u32x2 oe[2][4];
#pragma unroll
      for (int h = 0; h < 4; h++) {
        const int hh = h & 1; const unsigned base = L + G_KVF + (h >> 1) * 2048, zero = L + G_KVF + 4096;
        const int col = (p4 == 0) ? 8 + 4 * hh + q4 : 4 * hh;
        const u32x4 e0 = cat(ld64(base + lane * 16, 8 * hh), ld64(g == h ? base + 1024 + (16 * hh + r16) * 16 + 8 : zero, 0));
        const u32x4 e1 = cat(ld64(base + 1024 + (16 * g + col) * 16, 0), ld64(g == h ? base + 1024 + (32 + 16 * hh + col) * 16 + 8 : zero, 0));
#pragma unroll
        for (int m = 0; m < 2; m++) {
          const u32x4 bq = cat(pq[m][h], pq[m][4]);
          const f32x4 n0 = mfma16(e0, bq, (f32x4){0, 0, 0, 0}), n1 = mfma16(e1, bq, (f32x4){0, 0, 0, 0});
          const float rc = __builtin_amdgcn_rcpf(n1[1] + 1e-6f);
          oe[m][h][0] = pk(n0[0] * rc, n0[1] * rc); oe[m][h][1] = pk(n0[2] * rc, n0[3] * rc); tl[m][h] = n1[0] * rc;
        }
      }
#pragma unroll
      for (int m = 0; m < 2; m++) {
        const unsigned so = WS_O + (rb * 8 + lw * 2 + m) * 20480 + (cg >> 1) * 5120;
        gst128c(rws, lane * 16, so + (cg & 1) * 2048, cat(oe[m][0], oe[m][1]));
        gst128c(rws, lane * 16, so + (cg & 1) * 2048 + 1024, cat(oe[m][2], oe[m][3]));
        gst64c(rws, lane * 8, so + 4096 + (cg & 1) * 512, (u32x2){pk(tl[m][0], tl[m][1]), pk(tl[m][2], tl[m][3])});
      }
      ST(12);
      WAITV(0);
      if (lane == 0) set_flag(ws, FL_O + (rb * 4 + lw) * 8 + cg, tag);
      ST(4);
    } else {
      tag = __hip_atomic_load(pe, __ATOMIC_RELAXED, __HIP_MEMORY_SCOPE_AGENT) + 1u;
      const int rg = lw >> 1, c = lw & 1;
      unsigned aA[2]; { const int row = rg * 64 + r16;
#pragma unroll
        for (int kk = 0; kk < 2; kk++) aA[kk] = L + row * 128 + (((4 * kk + g) ^ ((row >> 1) & 7)) << 4); }
      unsigned aB[5];
#pragma unroll
      for (int n = 0; n < 5; n++) { int sec, off; if (n < 4) { sec = n >> 1; off = dl + 80 * c + 40 * (n & 1) + 8 * p4; } else { sec = p4 >> 1; off = dl + 80 * c + 40 * (p4 & 1) + 32; }
        off ^= ((g & 1) << 5) ^ (sec << 4); aB[n] = L + XB + sec * WB + (8 * g + q4) * 192 + off; }
      if (lane == 0) st128(L + G_KVF + 4096, 0, (u32x4){0, 0, 0, 0});
      f32x4 acc[4][5];
      u32x4 fa[2][4]; u32x2 fb[2][5][2];
#pragma unroll
      for (int m = 0; m < 4; m++) { fa[1][m] = (u32x4){0, 0, 0, 0};
#pragma unroll
        for (int n = 0; n < 5; n++) acc[m][n] = (f32x4){0, 0, 0, 0}; }
#pragma unroll
      for (int n = 0; n < 5; n++) { fb[1][n][0] = (u32x2){0, 0}; fb[1][n][1] = (u32x2){0, 0}; }
      int sl = 0, d = 0;
      auto half = [&](int rbuf, int kk) {
#pragma unroll
        for (int t = 0; t < 20; t++) {
          const int n = t >> 2, m = t & 3;
          acc[m][n] = mfma16(fa[1 - rbuf][m], cat(fb[1 - rbuf][n][0], fb[1 - rbuf][n][1]), acc[m][n]);
          SCHED();
          if (t < 4) fa[rbuf][t] = ld128(aA[kk], t * 2048);
          else if (t < 14) { const int n2 = (t - 4) >> 1, h = (t - 4) & 1; fb[rbuf][n2][h] = ldtr(aB[n2], kk * 32 * 192 + h * 768); }
          else if (kk == 0) { if (t == 14) { d = (sl == 2) ? -2 * SLOT : SLOT; sl = (sl == 2) ? 0 : sl + 1; } if (t == 15) { aA[0] += d; asm volatile("" : "+v"(aA[0])); } }
          else { if (t == 14) { aA[1] += d; aB[0] += d; asm volatile("" : "+v"(aA[1]), "+v"(aB[0])); } if (t == 15) { aB[1] += d; aB[2] += d; asm volatile("" : "+v"(aB[1]), "+v"(aB[2])); } if (t == 16) { aB[3] += d; aB[4] += d; asm volatile("" : "+v"(aB[3]), "+v"(aB[4])); } }
          SCHED();
        }
      };
      asm volatile("" : "+v"(tag));
      WAITL0();
#pragma unroll 1
      for (int s = 0; s < 10; s++) { SCHED(); WAITL0(); BAR(); SCHED(); half(0, 0); half(1, 1); }
      WAITL0(); BAR(); SCHED();
#pragma unroll
      for (int n = 0; n < 5; n++)
#pragma unroll
        for (int m = 0; m < 4; m++) acc[m][n] = mfma16(fa[1][m], cat(fb[1][n][0], fb[1][n][1]), acc[m][n]);
      ST(1);
      f32x4 R[7];
#pragma unroll
      for (int i = 0; i < 7; i++) R[i] = (f32x4){0, 0, 0, 0};
#pragma unroll
      for (int ks = 0; ks < 2; ks++) {
        u32x4 T[6];
#pragma unroll
        for (int hf = 0; hf < 2; hf++) { const int m = 2 * ks + hf;
#pragma unroll
          for (int e = 0; e < 2; e++) {
            T[0][2 * hf + e] = pk(phi(acc[m][0][2 * e]), phi(acc[m][0][2 * e + 1]));
            T[1][2 * hf + e] = pk(phi(acc[m][1][2 * e]), phi(acc[m][1][2 * e + 1]));
            T[2][2 * hf + e] = pk(acc[m][2][2 * e], acc[m][2][2 * e + 1]);
            T[3][2 * hf + e] = pk(acc[m][3][2 * e], acc[m][3][2 * e + 1]);
            T[4][2 * hf + e] = pk(phi(acc[m][4][2 * e]), phi(acc[m][4][2 * e + 1]));
            T[5][2 * hf + e] = r16 < 8 ? 0x3f803f80u : pk(acc[m][4][2 * e], acc[m][4][2 * e + 1]);
          } }
        R[0] = mfma16(T[0], T[2], R[0]); R[1] = mfma16(T[1], T[3], R[1]);
        R[2] = mfma16(T[0], T[5], R[2]); R[3] = mfma16(T[1], T[5], R[3]);
        R[4] = mfma16(T[4], T[2], R[4]); R[5] = mfma16(T[4], T[3], R[5]); R[6] = mfma16(T[4], T[5], R[6]);
      }
      f32x4 M23, M456;
#pragma unroll
      for (int i = 0; i < 4; i++) {
        M23[i] = (q4 & 1) ? R[3][i] : R[2][i];
        const float r45 = (g == 0) ? R[4][i] : R[5][i];
        M456[i] = __uint_as_float(__builtin_amdgcn_permlane32_swap(__float_as_uint(r45), __float_as_uint(R[6][i]), false, false)[0]);
      }
      f32x4 k0 = rg ? M23 : R[0], k1 = rg ? M456 : R[1];
      { const f32x4 s0 = rg ? R[0] : M23, s1 = rg ? R[1] : M456;
        st128(L + G_RED + lw * 2048 + lane * 16, 0, __builtin_bit_cast(u32x4, s0)); st128(L + G_RED + lw * 2048 + lane * 16, 1024, __builtin_bit_cast(u32x4, s1)); }
      ST(8); WAITL0(); BAR(); ST(9);
      k0 += __builtin_bit_cast(f32x4, ld128(L + G_RED + (lw ^ 2) * 2048 + lane * 16, 0));
      k1 += __builtin_bit_cast(f32x4, ld128(L + G_RED + (lw ^ 2) * 2048 + lane * 16, 1024));
      const int irow = c * 2 + rg;
      gst128c(rws, lane * 16, WS_PART + (cg * 16 + rb) * 4096 + irow * 1024, (u32x4){pk(k0[0], k0[1]), pk(k0[2], k0[3]), pk(k1[0], k1[1]), pk(k1[2], k1[3])});
      ST(10);
      WAITV(0);
      if (lane == 0) set_flag(ws, FL_P + (cg * 16 + rb) * 4 + irow, tag);
      ST(2);
      wait_flags(ws, FL_P + (cg * 16 + r16) * 4 + irow, tag);
      ST(3);
      u32x4 pv[4][4];
#pragma unroll
      for (int blk = 0; blk < 4; blk++)
#pragma unroll
        for (int rp = 0; rp < 4; rp++) pv[blk][rp] = gld128c(rws, g * 4096 + r16 * 16 + blk * 256, WS_PART + (cg * 16 + rp * 4) * 4096 + irow * 1024);
      u32x4 sel[2];
#pragma unroll
      for (int o = 0; o < 2; o++)
#pragma unroll
        for (int i = 0; i < 4; i++) sel[o][i] = ((r16 >> 1) == 4 * o + i) ? ((r16 & 1) ? 0x3f800000u : 0x00003f80u) : 0u;
#pragma unroll
      for (int bp = 0; bp < 2; bp++) {
        f32x4 sum = {0, 0, 0, 0};
#pragma unroll
        for (int o = 0; o < 2; o++)
#pragma unroll
          for (int rp = 0; rp < 4; rp++) sum = mfma16(sel[o], pv[2 * bp + o][rp], sum);
        st64(L + G_KVF + irow * 1024 + ((2 * bp + (g >> 1)) * 16 + r16) * 16 + (g & 1) * 8, 0, (u32x2){pk(sum[0], sum[1]), pk(sum[2], sum[3])});
      }
      ST(11); WAITL0(); BAR();
      ST(4);
      const i32x4 so_ = make_srd(Bo, 640 * 640 * 2);
      unsigned vo[3];
#pragma unroll
      for (int u = 0; u < 3; u++) { const unsigned cc = u * 64 + lane, row = (cc * 171) >> 11, pos = cc - row * 12; vo[u] = row * 1280 + ((pos ^ (((row >> 2) & 1) << 1)) << 4); }
#pragma unroll 1
      for (int i = 0; i < 10; i++) { const int gr = i * 4 + lw;
#pragma unroll
        for (int u = 0; u < 3; u++) dma16(so_, vo[u], gr * 16 * 1280 + a0, L + (gr * 3 + u) * 1024); }
      ST(12); WAITV(0); ST(13);
    }
    wait_flags(ws, FL_O + (rb * 4 + (wv >> 1)) * 8 + (lane & 7), tag);
    ST(5);
    u32x4 of[20];
#pragma unroll
    for (int p = 0; p < 4; p++) {
      const unsigned so = WS_O + (rb * 8 + wv) * 20480 + p * 5120;
#pragma unroll
      for (int f = 0; f < 4; f++) of[5 * p + f] = gld128c(rws, lane * 16 + f * 1024, so);
      of[5 * p + 4] = cat(gld64c(rws, lane * 8, so + 4096), gld64c(rws, lane * 8 + 512, so + 4096));
    }
    if (b == 0 && tid == 0) __hip_atomic_store(pe, tag, __ATOMIC_RELAXED, __HIP_MEMORY_SCOPE_AGENT);
    BAR();
    ST(14);
    unsigned aW[2][2], aT[2];
#pragma unroll
    for (int e = 0; e < 2; e++) {
#pragma unroll
      for (int par = 0; par < 2; par++) aW[par][e] = L + (4 * g + q4) * 192 + ((dl + 32 * e + 8 * p4) ^ (((g + par) & 1) << 5));
      aT[e] = L + (20 * q4 + 16 + g) * 192 + ((dl + 32 * e + 8 * p4) ^ ((q4 & 1) << 5));
    }
    f32x4 oa[5];
#pragma unroll
    for (int n = 0; n < 5; n++) oa[n] = (f32x4){0, 0, 0, 0};
#pragma unroll
    for (int p = 0; p < 4; p++) {
#pragma unroll
      for (int f = 0; f < 5; f++)
#pragma unroll
        for (int n = 0; n < 5; n++) {
          const int co = (n >> 1) * 64;
          u32x4 w;
          if (f < 4) w = cat(ldtr(aW[0][n & 1], f * 40 * 192 + co), ldtr(aW[1][n & 1], (f * 40 + 20) * 192 + co));
          else w = cat(ldtr(aT[n & 1], co), ldtr(aT[n & 1], 80 * 192 + co));
          oa[n] = mfma16(w, of[5 * p + f], oa[n]);
        }
#pragma unroll
      for (int e = 0; e < 2; e++) { aW[0][e] += 160 * 192; aW[1][e] += 160 * 192; aT[e] += 160 * 192; }
    }
    ST(6);
    const unsigned so = L + G_OUT + wv * 2560;
#pragma unroll
    for (int n = 0; n < 5; n++) st64(so + r16 * 160 + g * 8, n * 32, (u32x2){pk(oa[n][0], oa[n][1]), pk(oa[n][2], oa[n][3])});
    const rsrc_t ro = mk(out, 2048 * 1280 * 2);
#pragma unroll
    for (int u = 0; u < 3; u++) { const unsigned cc = u * 64 + lane, row = (cc * 205) >> 11;
      if (u < 2 || lane < 32) __builtin_amdgcn_raw_buffer_store_b128(ld128(so + cc * 16, 0), ro, row * 2400 + cc * 16, (rb * 128 + wv * 16) * 2560 + 1280 + cg * 160, 16); }
    ST(7); STEND();
  } else {
    const int xg = xcd;
    const i32x4 sk_ = make_srd(Ak, 2048 * 1280), sv_ = make_srd(Av, 2048 * 1280);
    unsigned vr[3];
#pragma unroll
    for (int u = 0; u < 3; u++) { const unsigned c = tid + 512 * u, row = (c * 6554) >> 16; vr[u] = (128 * j + row) * 1280 + (c - row * 10) * 16 + xg * 160; }
#pragma unroll
    for (int u = 0; u < 3; u++) {
      if (u < 2 || wv < 5) dma16(sk_, vr[u] - 4 * 1280, 0, L + A_K + (u * 8 + wv) * 1024);
      if (u < 2 || wv < 4) dma16(sv_, vr[u], 0, L + A_V + (u * 8 + wv) * 1024);
    }
    const unsigned tag = __hip_atomic_load(pe, __ATOMIC_RELAXED, __HIP_MEMORY_SCOPE_AGENT) + 1u;
    u32x2 wk[3];
#pragma unroll
    for (int u = 0; u < 3; u++) { const int tk = (wv + 8 * u) % 5; wk[u] = *(const GAS u32x2*)(Ask + (80 * xg + 16 * tk + r16) * 4); }
    const rsrc_t rh = mk(Ah, 640 * 4096);
    u32x4 hv[3]; u32x2 hp[3];
#pragma unroll
    for (int u = 0; u < 3; u++) if (u < 2 || wv < 4) { const unsigned c = tid + 512 * u;
      hv[u] = gld128(rh, c * 16, (80 * xg + 5 * j) * 4096);
      hp[u] = __builtin_amdgcn_raw_buffer_load_b64(rh, (c & 255) ? (80 * xg + 5 * j) * 4096 + c * 16 - 8 : 0xfffffff0u, 0, 0); }
    if (tid < 480) { const unsigned ar = (tid * 2731) >> 16; st128(L + ar * HCP + (tid - ar * 24) * 16, 0, (u32x4){0, 0, 0, 0}); }
    WAITV(0); BAR();
    ST(1);
#pragma unroll
    for (int u = 0; u < 3; u++) if (u < 2 || wv < 4) {
      const int tk = wv + 8 * u, sq = (tk * 13) >> 6, ct = tk - 5 * sq, un = 4 * sq + g;
      const unsigned ad = L + (8 * un + q4) * 160 + 32 * ct + 8 * p4;
      const u32x2 k0 = ldtr(ad, A_K), k1 = ldtr(ad, A_K + 640), k2 = ldtr(ad, A_K + 1280), v0 = ldtr(ad + A_V, 0), v1 = ldtr(ad + A_V, 640);
      const float kf[11] = {bhi(k0[0]), blo(k0[1]), bhi(k0[1]), blo(k1[0]), bhi(k1[0]), blo(k1[1]), bhi(k1[1]), blo(k2[0]), bhi(k2[0]), blo(k2[1]), bhi(k2[1])};
      const float vf[8] = {blo(v0[0]), bhi(v0[0]), blo(v0[1]), bhi(v0[1]), blo(v1[0]), bhi(v1[0]), blo(v1[1]), bhi(v1[1])};
      const float w0 = blo(wk[u][0]), w1 = bhi(wk[u][0]), w2 = blo(wk[u][1]), w3 = bhi(wk[u][1]);
      float o[8];
#pragma unroll
      for (int i = 0; i < 8; i++) o[i] = (kf[i] * w3 + kf[i + 1] * w2 + kf[i + 2] * w1 + kf[i + 3] * w0) * vf[i];
      gst128c(rws, (255 - 16 * j - un) * 1280 + (16 * ct + r16) * 16, WS_KVT + xg * 327680, (u32x4){pk(o[7], o[6]), pk(o[5], o[4]), pk(o[3], o[2]), pk(o[1], o[0])});
    }
    ST(8);
#pragma unroll
    for (int u = 0; u < 3; u++) if (u < 2 || wv < 4) { const unsigned c = tid + 512 * u;
      const unsigned ad = L + (c >> 8) * HCH + 384 + (c & 255) * 16;
      const unsigned e0 = __builtin_amdgcn_alignbyte(hp[u][1], hp[u][0], 2), e1 = __builtin_amdgcn_alignbyte(hv[u][0], hp[u][1], 2), e2 = __builtin_amdgcn_alignbyte(hv[u][1], hv[u][0], 2),
                     e3 = __builtin_amdgcn_alignbyte(hv[u][2], hv[u][1], 2), e4 = __builtin_amdgcn_alignbyte(hv[u][3], hv[u][2], 2);
      st128(ad, 0, hv[u]); st128(ad, HCP, (u32x4){e1, e2, e3, e4}); st128(ad, 2 * HCP, (u32x4){hp[u][1], hv[u][0], hv[u][1], hv[u][2]}); st128(ad, 3 * HCP, (u32x4){e0, e1, e2, e3}); }
    ST(9);
    WAITV(0); WAITL0(); BAR();
    if (tid == 0) set_flag(ws, FL_K + xg * 16 + j, tag);
    ST(2);
    if (tid < 40) st128(L + A_X + (tid >> 3) * XCH + 4096 + (tid & 7) * 16, 0, (u32x4){0, 0, 0, 0});
    const int chl = wv < 4 ? wv : 4;
    const float Dc = __uint_as_float((unsigned)AD[80 * xg + 5 * j + chl] << 16);
    if (wv == 0) wait_flags(ws, FL_K + xg * 16 + r16, tag);
    BAR();
    ST(3);
    { u32x4 xv[3]; unsigned xa[3];
#pragma unroll
      for (int u = 0; u < 3; u++) if (u < 2 || wv < 4) { const unsigned c = tid + 512 * u, blk = (c * 13108) >> 16, ch = c - 5 * blk;
        xv[u] = gld128c(rws, blk * 1280 + ch * 16, WS_KVT + xg * 327680 + j * 80); xa[u] = L + A_X + ch * XCH + blk * 16; }
      if (j == 0 && tid == 0) __hip_atomic_store(pe, tag, __ATOMIC_RELAXED, __HIP_MEMORY_SCOPE_AGENT);
#pragma unroll
      for (int u = 0; u < 3; u++) if (u < 2 || wv < 4) st128(xa[u], 0, xv[u]); }
    const rsrc_t rq = mk(Aq, 2048 * 1280);
    u32x4 qv[3];
#pragma unroll
    for (int u = 0; u < 3; u++) if (u < 2 || wv < 4) qv[u] = gld128(rq, vr[u], 0);
    WAITL0(); BAR();
    ST(4);
    unsigned aH = L + chl * HCH + (3 - p4) * HCP + (64 * q4 + 8 * g) * 2;
    unsigned aXr = L + A_X + chl * XCH + (1852 - 1792 - 4 * r16 + 8 * g) * 2;
    const unsigned aE = L + A_X + chl * XCH + (2044 - 1792 - 64 * g - 4 * r16) * 2;
    const unsigned yo = WS_YT + (80 * xg + 5 * j + chl) * 4096;
    auto fin = [&](f32x4 a, u32x2 xr, unsigned off) {
      gst64c(rws, lane * 8 + off, yo, (u32x2){pk(a[0] + Dc * bhi(xr[1]), a[1] + Dc * blo(xr[1])), pk(a[2] + Dc * bhi(xr[0]), a[3] + Dc * blo(xr[0]))}); };
    if (wv < 4) {
      f32x4 acc[8];
      u32x4 F[2][8]; u32x2 G[2][8][2];
#pragma unroll
      for (int t = 0; t < 8; t++) { acc[t] = (f32x4){0, 0, 0, 0}; F[0][t] = ld128(aH, 512 * t); G[0][t][0] = ld64(aXr, 512 * (7 - t)); G[0][t][1] = ld64(aXr, 512 * (7 - t) + 8); }
      auto step = [&](int cur, int off) {
        int cnt = 0;
#pragma unroll
        for (int t = 0; t < 8; t++)
#pragma unroll
          for (int p = 0; p <= t; p++) {
            const int tt = t, pp = p;
            acc[tt] = mfma16(F[cur][pp], cat(G[cur][tt - pp][0], G[cur][tt - pp][1]), acc[tt]);
            SCHED();
            if (cnt < 24) { const int i = cnt / 3, w = cnt % 3;
              if (w == 0) F[1 - cur][i] = ld128(aH, 512 * i + off); else G[1 - cur][i][w - 1] = ld64(aXr, 512 * (7 - i) + off + 8 * (w - 1)); }
            if (cnt == 24 && cur == 1) { aH += 128; aXr += 128; asm volatile("" : "+v"(aH), "+v"(aXr)); }
            cnt++;
            SCHED();
          }
      };
#pragma unroll 1
      for (int r = 0; r < 4; r++) { step(0, 64); step(1, 128); }
      ST(10);
#pragma unroll
      for (int t = 0; t < 8; t++) fin(acc[t], ld64(aE, 512 * (7 - t)), 512 * t);
    } else {
#pragma unroll 1
      for (int z = 0; z < 2; z++) {
        const int t = z ? lw : 7 - lw;
        unsigned h2 = aH, x2 = aXr + 512 * (7 - t);
        f32x4 a0_ = {0, 0, 0, 0}, a1_ = {0, 0, 0, 0};
#pragma unroll 1
        for (int m = 0; m <= t; m++) {
#pragma unroll
          for (int r = 0; r < 8; r += 2) {
            a0_ = mfma16(ld128(h2, 64 * r), cat(ld64(x2, 64 * r), ld64(x2, 64 * r + 8)), a0_);
            a1_ = mfma16(ld128(h2, 64 * r + 64), cat(ld64(x2, 64 * r + 64), ld64(x2, 64 * r + 72)), a1_);
          }
          h2 += 512; x2 += 512;
        }
        fin(a0_ + a1_, ld64(aE + 512 * (7 - t), 0), 512 * t);
      }
    }
    ST(5);
    WAITV(0); WAITL0(); BAR();
    if (tid == 0) set_flag(ws, FL_Y + xg * 16 + j, tag);
    ST(11);
    if (wv == 0) wait_flags(ws, FL_Y + xg * 16 + r16, tag);
    BAR();
    ST(6);
    { u32x4 yv[3];
#pragma unroll
      for (int u = 0; u < 3; u++) if (u < 2 || wv < 4) yv[u] = gld128c(rws, ((tid >> 4) + 32 * u) * 4096 + r16 * 16, WS_YT + 80 * xg * 4096 + j * 256);
      const unsigned ad = L + r16 * (8 * 160 + 16) + (tid >> 4) * 2;
#pragma unroll
      for (int u = 0; u < 3; u++) if (u < 2 || wv < 4) {
#pragma unroll
        for (int e = 0; e < 4; e++) { st16(ad, 64 * u + 320 * e, (u16)yv[u][e]); st16(ad, 64 * u + 320 * e + 160, (u16)(yv[u][e] >> 16)); } } }
    WAITL0(); BAR();
    ST(12);
    const rsrc_t ro = mk(out, 2048 * 1280 * 2);
#pragma unroll
    for (int u = 0; u < 3; u++) if (u < 2 || wv < 4) { const unsigned c = tid + 512 * u, row = (c * 6554) >> 16;
      const u32x4 y = ld128(L + c * 16 + (row >> 3) * 16, 0); u32x4 r;
#pragma unroll
      for (int e = 0; e < 4; e++) r[e] = pk(blo(y[e]) * blo(qv[u][e]), bhi(y[e]) * bhi(qv[u][e]));
      __builtin_amdgcn_raw_buffer_store_b128(r, ro, vr[u] + (128 * j + row) * 1280, 0, 16); }
    ST(7); STEND();
  }
}

__global__ void spin_small(int us){ const u64 t0=__builtin_amdgcn_s_memrealtime(); while(__builtin_amdgcn_s_memrealtime()<t0+100ull*us) __builtin_amdgcn_s_sleep(1); }
__global__ void __launch_bounds__(512,2) spin_big(int us,unsigned* sink){ __shared__ char sm[163840]; const u64 t0=__builtin_amdgcn_s_memrealtime(); asm volatile("v_mov_b32 v220, 0":::"v220"); if(us<0){ sm[threadIdx.x]=1; __syncthreads(); sink[0]=sm[5]; } while(__builtin_amdgcn_s_memrealtime()<t0+100ull*us) __builtin_amdgcn_s_sleep(1); }
extern "C" void kernel_launch(void* const* d_in, const int* in_sizes, int n_in, void* d_out, int out_size, void* d_ws, size_t ws_size, hipStream_t stream) {
  hipMemsetAsync((char*)d_ws+(32<<20),0,4096,stream);
  fused_kernel<<<256, 512, 0, stream>>>((const u16*)d_in[0], (const u16*)d_in[1], (const u16*)d_in[2], (const u16*)d_in[3], (const u16*)d_in[4], (const u16*)d_in[5],
                                        (const u16*)d_in[6], (const u16*)d_in[7], (const u16*)d_in[8], (u16*)d_out, (char*)d_ws);
}
```

```cpp
#include <hip/hip_runtime.h>
#include <hip/hip_bf16.h>

typedef unsigned short u16;
typedef unsigned long long u64;
typedef short s16x4 __attribute__((ext_vector_type(4)));
typedef short s16x8 __attribute__((ext_vector_type(8)));
typedef __bf16 bf16x2 __attribute__((ext_vector_type(2)));
typedef __bf16 bf16x8 __attribute__((ext_vector_type(8)));
typedef float f32x2 __attribute__((ext_vector_type(2)));
typedef float f32x4 __attribute__((ext_vector_type(4)));
typedef unsigned u32x2 __attribute__((ext_vector_type(2)));
typedef unsigned u32x4 __attribute__((ext_vector_type(4)));
typedef int i32x4 __attribute__((ext_vector_type(4)));
#define LAS __attribute__((address_space(3)))
#define GAS __attribute__((address_space(1)))
#define DI __device__ __forceinline__
#define WAITV(n) asm volatile("s_waitcnt vmcnt(" #n ")" ::: "memory")
#define WAITL0() __builtin_amdgcn_s_waitcnt(0xC07F)
#define WAITL(n) __builtin_amdgcn_s_waitcnt(0xC07F | ((n) << 8))
#define BAR() do { asm volatile("" ::: "memory"); __builtin_amdgcn_s_barrier(); asm volatile("" ::: "memory"); } while (0)
#define SCHED() __builtin_amdgcn_sched_barrier(0)
#define P1 2
#define P2 5
#define P3 8
#define HPOS 1
#define QPRE 0
#define OUTAUX 16
#define CODEB 30720
#define STAMP 0

DI f32x4 mfma16(u32x4 a, u32x4 b, f32x4 c) { return __builtin_amdgcn_mfma_f32_16x16x32_bf16(__builtin_bit_cast(bf16x8, a), __builtin_bit_cast(bf16x8, b), c, 0, 0, 0); }
DI u32x4 cat(u32x2 a, u32x2 b) { return __builtin_shufflevector(a, b, 0, 1, 2, 3); }
DI u32x4 ld128(unsigned a, int off) { return *(const LAS u32x4*)((LAS char*)(size_t)a + off); }
DI u32x2 ld64(unsigned a, int off) { return *(const LAS u32x2*)((LAS char*)(size_t)a + off); }
DI u32x2 ldtr(unsigned a, int off) { return __builtin_bit_cast(u32x2, __builtin_amdgcn_ds_read_tr16_b64_v4i16((LAS s16x4*)((LAS char*)(size_t)a + off))); }
DI void st128(unsigned a, int off, u32x4 v) { *(LAS u32x4*)((LAS char*)(size_t)a + off) = v; }
DI void st64(unsigned a, int off, u32x2 v) { *(LAS u32x2*)((LAS char*)(size_t)a + off) = v; }
DI void st16(unsigned a, int off, u16 v) { *(LAS u16*)((LAS char*)(size_t)a + off) = v; }
DI unsigned pk(float lo, float hi) { f32x2 v = {lo, hi}; return __builtin_bit_cast(unsigned, __builtin_convertvector(v, bf16x2)); }
DI float blo(unsigned u) { return __uint_as_float(u << 16); }
DI float bhi(unsigned u) { return __uint_as_float(u & 0xffff0000u); }
DI unsigned phi2(float u0, float u1) {
  const f32x2 u = {u0, u1}, a = u * 1.44269504088896341f - 1.44269504088896341f;
  f32x2 p = {__builtin_amdgcn_fmed3f(u0, __builtin_amdgcn_exp2f(a[0]), 1.f), __builtin_amdgcn_fmed3f(u1, __builtin_amdgcn_exp2f(a[1]), 1.f)};
  p += 1e-6f;
  return pk(p[0], p[1]);
}
DI float add1(float a, float b) { float r = a + b; asm("" : "+v"(r)); return r; }
DI float mul1(float a, float b) { float r = a * b; asm("" : "+v"(r)); return r; }
DI float fma1(float a, float b, float c) { float r = __builtin_fmaf(a, b, c); asm("" : "+v"(r)); return r; }
#define MFMA_NEW(acc, a, b) asm("v_mfma_f32_16x16x32_bf16 %0, %1, %2, 0" : "=&v"(acc) : "v"(a), "v"(b))
#define MFMA_ACC(acc, a, b) asm("v_mfma_f32_16x16x32_bf16 %0, %1, %2, %0" : "+v"(acc) : "v"(a), "v"(b))
DI i32x4 make_srd(const void* p, unsigned bytes) { u64 a = (u64)p; i32x4 r; r[0] = (int)(unsigned)a; r[1] = (int)(unsigned)(a >> 32) & 0xffff; r[2] = (int)bytes; r[3] = 0x00020000; return r; }
DI void dma16(i32x4 srd, unsigned voff, unsigned soff, unsigned m0v) {
  asm volatile("s_mov_b32 m0, %3\n\ts_nop 0\n\tbuffer_load_dwordx4 %0, %1, %2 offen lds" ::"v"(voff), "s"(srd), "s"(soff), "s"(m0v) : "memory");
}
DI void touch(i32x4 srd, unsigned voff, unsigned soff, unsigned m0v) {
  asm volatile("s_mov_b32 m0, %3\n\ts_nop 0\n\tbuffer_load_dword %0, %1, %2 offen lds" ::"v"(voff), "s"(srd), "s"(soff), "s"(m0v) : "memory");
}
typedef __amdgpu_buffer_rsrc_t rsrc_t;
DI rsrc_t mk(const void* p, unsigned bytes) { return __builtin_amdgcn_make_buffer_rsrc((void*)p, 0, bytes, 0x00020000); }
constexpr int SC1 = 16;
DI u32x4 gld128(rsrc_t r, unsigned v, unsigned s) { return __builtin_amdgcn_raw_buffer_load_b128(r, v, s, 0); }
DI u32x4 gld128c(rsrc_t r, unsigned v, unsigned s) { return __builtin_amdgcn_raw_buffer_load_b128(r, v, s, SC1); }
DI u32x2 gld64c(rsrc_t r, unsigned v, unsigned s) { return __builtin_amdgcn_raw_buffer_load_b64(r, v, s, SC1); }
DI void gst128c(rsrc_t r, unsigned v, u32x4 d) { __builtin_amdgcn_raw_buffer_store_b128(d, r, v, 0, SC1); }
DI void gst64c(rsrc_t r, unsigned v, unsigned s, u32x2 d) { __builtin_amdgcn_raw_buffer_store_b64(d, r, v, s, SC1); }

constexpr unsigned WS_EPOCH = 0;
constexpr unsigned WS_FLAG = 4096;
constexpr unsigned FL_G = 0;
constexpr unsigned FL_K = 128;
constexpr unsigned FL_A = 256;
constexpr unsigned WS_PART = 1u << 18;
constexpr unsigned WS_O = 1u << 20;
constexpr unsigned WS_KVT = 4u << 20;
constexpr unsigned WS_YT = 8u << 20;
constexpr unsigned WS_DBG = 12u << 20;
constexpr unsigned WS_BYTES = 16u << 20;

DI void set_flag(char* ws, unsigned idx, unsigned tag) { __hip_atomic_store((GAS unsigned*)(ws + WS_FLAG + idx * 128), tag, __ATOMIC_RELAXED, __HIP_MEMORY_SCOPE_AGENT); }
DI void wait_flags(char* ws, unsigned idx, unsigned tag) {
  GAS unsigned* p = (GAS unsigned*)(ws + WS_FLAG + idx * 128);
#pragma unroll 1
  for (int spins = 0; spins < (1 << 18); ++spins) {
    const unsigned v = __hip_atomic_load(p, __ATOMIC_RELAXED, __HIP_MEMORY_SCOPE_AGENT);
    if (__all(v == tag)) break;
    __builtin_amdgcn_s_sleep(1);
  }
  asm volatile("" ::: "memory");
}
constexpr unsigned EMPTY = 0xffffffffu;
constexpr int SWEEPS = 1 << 16;
DI unsigned umax3(unsigned a, unsigned b, unsigned c) { return __builtin_elementwise_max(__builtin_elementwise_max(a, b), c); }
DI unsigned mx4(unsigned m, u32x4 v) { return umax3(umax3(m, v[0], v[1]), v[2], v[3]); }
DI bool none_empty(unsigned m) { return __builtin_amdgcn_ballot_w64(m == EMPTY) == 0; }
#define PIN(x) asm volatile("" : "+v"(x))
#define ST(n)
#define STEND()

constexpr int SLOT = 40960, XB = 16384, WB = 12288;
constexpr int G_RED = 36864;
constexpr int L_DONE = 161792;
constexpr int L_CNT = 161808;
constexpr int A_DMP = 113920;
constexpr int L_DUMMY = 163584;
constexpr int T_SCR = 106496;
constexpr int WOA = 131072, WOB = 45056, WOC = 75776, WOD = 0;
constexpr int HCP = 4640, HCH = 4 * HCP;
constexpr int A_X = 5 * HCH, XCH = 4224;
constexpr int A_K = A_X, A_V = A_X + 23552;

__global__ void __launch_bounds__(512, 2)
fused_kernel(const u16* __restrict__ Aq, const u16* __restrict__ Ak, const u16* __restrict__ Av, const u16* __restrict__ Ask,
             const u16* __restrict__ Ah, const u16* __restrict__ AD, const u16* __restrict__ Bx, const u16* __restrict__ Bw,
             const u16* __restrict__ Bo, u16* __restrict__ out, char* __restrict__ ws) {
  __shared__ __attribute__((aligned(16))) char smem[163840];
  const int b = blockIdx.x, xcd = b & 7, j = (b >> 3) & 15;
  int role = b >> 7;
  const int tid = threadIdx.x, lane = tid & 63;
  int wv = __builtin_amdgcn_readfirstlane(tid >> 6);
  const unsigned L = (unsigned)(size_t)(LAS char*)smem;
  unsigned stampv = 0; (void)stampv;
  ST(0);
  asm volatile("" ::"s"(Aq), "s"(Ak), "s"(Av), "s"(Ask), "s"(Ah), "s"(AD), "s"(Bx), "s"(Bw), "s"(Bo), "s"(out), "s"(ws));
  const int rb = (xcd >> 1) * 4 + (j >> 2), cg = (xcd & 1) * 4 + (j & 3);
  const int a0 = (160 * cg) & ~63, dl = 160 * cg - a0;
  const int hf = xcd & 1, ai = (xcd >> 1) * 16 + j;
  const i32x4 sx = make_srd(Bx, 2048 * 640 * 2), sw = make_srd(Bw, 640 * 1920 * 2);

  unsigned va[3], vb[3], vx = 0;
  int sp = 0;
  u32x2 wk[3];
  auto issue_x = [&](int slot) {
#pragma unroll
    for (int u = 0; u < 4; u++) dma16(sx, vx, sp * 128 + u * 32 * 1280, L + slot * SLOT + ((wv & 3) + 4 * u) * 1024);
  };
  auto issue_w = [&](int slot) {
    const int lw = wv & 3; const unsigned lb = L + slot * SLOT, so = (sp * 64 + 16 * lw) * 3840 + 1280 + a0;
#pragma unroll
    for (int u = 0; u < 3; u++) dma16(sw, va[u], so, lb + XB + (lw * 3 + u) * 1024);
#pragma unroll
    for (int u = 0; u < 3; u++) dma16(sw, vb[u], so + 1280, lb + XB + WB + (lw * 3 + u) * 1024);
    sp++;
  };
  auto warm = [&](int s) { touch(sx, va[0], s * 128, L + L_DUMMY); touch(sw, va[1], s * 64 * 3840 + 1280 + (xcd & 1) * 640, L + L_DUMMY); };
  const rsrc_t rws = mk(ws, WS_BYTES);
  unsigned tag = 0;
  if (wv == 7) {
    tag = *(const __attribute__((address_space(4))) unsigned*)(ws + WS_EPOCH) + 1u;
    if (role == 0) {
#pragma unroll
      for (int i = 0; i < 4; i++) gst128c(rws, WS_PART + (cg * 16 + rb) * 4096 + lane * 16 + i * 1024, (u32x4){EMPTY, EMPTY, EMPTY, EMPTY});
    }
    asm volatile("" : "+s"(tag));
  }
  if (role == 0) {
    if (wv < 4) {
      { const int row = 8 * wv + (lane >> 3); vx = (rb * 128 + row) * 1280 + (((lane & 7) ^ ((row >> 1) & 7)) << 4); }
      ST(13);
      issue_x(0);
#pragma unroll
      for (int u = 0; u < 3; u++) { const unsigned c = u * 64 + lane, row = (c * 171) >> 11, pos = c - row * 12; va[u] = row * 3840 + ((pos ^ ((row >> 3) << 1)) << 4); vb[u] = va[u] ^ 16; }
      issue_w(0);
    } else if (wv == 6) {
      u64 pc; asm volatile("s_getpc_b64 %0" : "=s"(pc));
      touch(make_srd((const void*)(pc & ~127ull), CODEB), lane < 16 ? (lane * 16 + j) * 128 : 0xfffffff0u, 0, L + L_DUMMY);
    }
  } else {
#pragma unroll
    for (int u = 0; u < 3; u++) asm volatile("global_load_dwordx2 %0, %1, %2" : "=v"(wk[u]) : "v"((320 * hf + 16 * ((wv + 8 * u) % 20) + (lane & 15)) * 8), "s"(Ask) : "memory");
    { const unsigned r = (lane * 26) >> 8, l = lane - r * 10;
      va[0] = (((xcd >> 1) * 512 + 32 * j + lane) * 1280) | (lane < 32 ? 0u : 0xf0000000u);
      va[1] = ((4 * j + r) * 3840 + (l >= 5 ? 640 : 0) + l * 128) | (lane < 40 ? 0u : 0xf0000000u); }
    if (wv < P1) warm(wv + 1);
    const i32x4 sk_ = make_srd(Ak, 2048 * 1280), sv_ = make_srd(Av, 2048 * 1280);
#pragma unroll
    for (int u = 0; u < 3; u++) { const unsigned c = tid + 512 * u, row = (c * 1639) >> 16;
      const unsigned v = (32 * ai + row) * 1280 + hf * 640 + (((c - row * 40) ^ (((row >> 2) & 6) | ((row >> 1) & 1))) << 4);
      if (u < 2 || wv < 7) dma16(sk_, v - 4 * 1280, 0, L + A_K + (u * 8 + wv) * 1024);
      if (u < 2 || wv < 4) dma16(sv_, v, 0, L + A_V + (u * 8 + wv) * 1024); }
    if (wv >= P1 && wv < P2) warm(wv + 1);
    ST(13);
  }
  asm volatile("" : "+s"(role), "+s"(wv));
  const int lw = wv & 3, g = lane >> 4, r16 = lane & 15, q4 = r16 >> 2, p4 = lane & 3;
  const rsrc_t ro = mk(out, 2048 * 1280 * 2);
  const i32x4 so_ = make_srd(Bo, 640 * 640 * 2);
  const int cg3 = j & 7, rt0 = (2 * xcd + (j >> 3)) * 8 + role * 4, a3 = (160 * cg3) & ~63, dl3 = 160 * cg3 - a3;
  unsigned vo[3];
  auto prep_wo = [&]() {
#pragma unroll
    for (int u = 0; u < 3; u++) { const unsigned cc = u * 64 + lane, row = (cc * 171) >> 11, ps = cc - row * 12; vo[u] = row * 1280 + ((ps ^ (((row >> 2) & 1) << 1)) << 4); }
  };
  const int psw = j & 1;
  auto load_wo = [&](int p, int pos, int first, int nw, int end) {
#pragma unroll 1
    for (int i = first; i < end; i += nw)
#pragma unroll
      for (int u = 0; u < 3; u++) dma16(so_, vo[u], ((p ^ psw) * 160 + i * 16) * 1280 + a3, L + pos + (i * 3 + u) * 1024);
  };

  if (role == 0) {
    if (wv < 4) {
      auto issue_q = [&](int pos) {
        const unsigned so = ((sp - 10) * 64 + 16 * lw) * 3840 + a0;
#pragma unroll
        for (int u = 0; u < 3; u++) dma16(sw, va[u], so, L + pos + (lw * 3 + u) * 1024);
        sp++;
      };
      sp = 1;
      PIN(vb[0]); PIN(vb[1]); PIN(vb[2]);
      issue_x(1); issue_w(1);
      ST(15);
      unsigned aX[2]; { const int row = lw * 32 + r16;
#pragma unroll
        for (int kk = 0; kk < 2; kk++) aX[kk] = L + row * 128 + (((4 * kk + g) ^ ((row >> 1) & 7)) << 4); }
      u32x4 xq[10][2][2];
      constexpr int QP[10] = {45056, 45056 + WB, 45056 + 2 * WB, 81920, 81920 + WB, 81920 + 2 * WB, 81920 + 3 * WB, 0, WB, 2 * WB};
#pragma unroll
      for (int s = 0; s < 10; s++) {
        if (s < 9) WAITV(10); else WAITV(9);
        WAITL0(); BAR();
        if (s < 8) issue_x((s + 2) % 3); else issue_q(QP[s == 8 ? 0 : 3]);
#pragma unroll
        for (int kk = 0; kk < 2; kk++)
#pragma unroll
          for (int m = 0; m < 2; m++) xq[s][kk][m] = ld128(aX[kk] + (s % 3) * SLOT, m * 2048);
        if (s < 8) issue_w((s + 2) % 3);
        else if (s == 8) { issue_q(QP[1]); issue_q(QP[2]); }
        else { issue_q(QP[4]); issue_q(QP[5]); issue_q(QP[6]); }
      }
      if (QPRE) WAITV(12);
      WAITL0(); BAR();
      issue_q(QP[7]); issue_q(QP[8]); issue_q(QP[9]);
      unsigned aQ[5];
#pragma unroll
      for (int n = 0; n < 5; n++) { const int off = (n < 4 ? dl + 40 * n + 8 * p4 : dl + 40 * p4 + 32) ^ ((g & 1) << 5); aQ[n] = L + QP[0] + (8 * g + q4) * 192 + off; }
      unsigned aE[4]; { const int col = p4 == 0 ? 8 + q4 : 0; aE[0] = L + G_RED + lane * 16; aE[1] = L + G_RED + r16 * 16; aE[2] = L + G_RED + (16 * g + col) * 16; aE[3] = L + G_RED + col * 16; }
      PIN(aE[0]); PIN(aE[1]); PIN(aE[2]); PIN(aE[3]);
      f32x4 qa[2][5];
#pragma unroll
      for (int m = 0; m < 2; m++)
#pragma unroll
        for (int n = 0; n < 5; n++) { qa[m][n] = (f32x4){1.f, 1.f, 1.f, 1.f}; asm volatile("" : "+v"(qa[m][n])); }
      ST(1);
      {
        u32x2 fw[2][5][2];
        auto rdw = [&](int ks, int n, int h) {
          if (h == 0 && ks == 6) { aQ[n] += QP[3] - QP[0]; asm volatile("" : "+v"(aQ[n])); }
          if (h == 0 && ks == 14) { aQ[n] -= QP[3]; asm volatile("" : "+v"(aQ[n])); }
          fw[ks & 1][n][h] = ldtr(aQ[n], (ks - (ks < 6 ? 0 : ks < 14 ? 6 : 14)) * 32 * 192 + 768 * h); };
        if (!QPRE) { ST(8); WAITV(0); BAR(); ST(10); }
#pragma unroll
        for (int n = 0; n < 5; n++) { rdw(0, n, 0); rdw(0, n, 1); }
        SCHED();
#pragma unroll
        for (int ks = 0; ks < 20; ks++) {
          if (QPRE && ks == 5) { ST(8); WAITV(0); WAITL0(); BAR(); ST(10); SCHED(); }
#pragma unroll
          for (int n = 0; n < 5; n++) {
            if (n == 0) WAITL(4); else if (n == 3) WAITL(6);
            SCHED();
            const u32x4 w = cat(fw[ks & 1][n][0], fw[ks & 1][n][1]);
            MFMA_ACC(qa[0][n], w, xq[ks >> 1][ks & 1][0]);
            SCHED();
            if (ks < 19) rdw(ks + 1, n, 0);
            SCHED();
            MFMA_ACC(qa[1][n], w, xq[ks >> 1][ks & 1][1]);
            SCHED();
            if (ks < 19) rdw(ks + 1, n, 1);
            SCHED();
          }
        }
        asm volatile("s_nop 7" : "+v"(qa[0][4]), "+v"(qa[1][4]), "+v"(qa[0][3]), "+v"(qa[1][3]), "+v"(qa[0][2]), "+v"(qa[1][2]), "+v"(qa[0][1]), "+v"(qa[1][1]), "+v"(qa[0][0]), "+v"(qa[1][0]));
      }
      ST(11);
      u32x4 bq[2][4];
#pragma unroll
      for (int m = 0; m < 2; m++) {
        const unsigned t0 = phi2(qa[m][4][0], qa[m][4][1]), t1 = phi2(qa[m][4][2], qa[m][4][3]);
#pragma unroll
        for (int h = 0; h < 4; h++) { bq[m][h] = (u32x4){phi2(qa[m][h][0], qa[m][h][1]), phi2(qa[m][h][2], qa[m][h][3]), g == h ? t0 : 0u, g == h ? t1 : 0u}; PIN(bq[m][h]); } }
      const unsigned ob = WS_O + (rb * 8 + lw * 2) * 20480 + (cg >> 1) * 5120;
      unsigned o16[2] = {ob + (cg & 1) * 2048 + lane * 16, ob + (cg & 1) * 2048 + lane * 16 + 20480}, o8 = ob + 4096 + (cg & 1) * 512 + lane * 8;
      f32x4 eps = {0, 1e-6f, 0, 0};
      PIN(o16[0]); PIN(o16[1]); PIN(o8); PIN(eps);
      ST(2);
      SCHED(); WAITL0(); BAR();
      ST(3);
      u32x2 e0[4][2], e1[4][2];
#pragma unroll
      for (int h = 0; h < 4; h++) { e1[h][0] = ld64(aE[2], (h >> 1) * 2048 + (h & 1) * 64); SCHED(); e1[h][1] = ld64(aE[3], (h >> 1) * 2048 + (h & 1) * 320 + 520); SCHED(); }
#pragma unroll
      for (int h = 0; h < 4; h++) { e0[h][0] = ld64(aE[0], 4096 + (h >> 1) * 2048 + 8 * (h & 1)); SCHED(); e0[h][1] = ld64(aE[1], (h >> 1) * 2048 + (h & 1) * 256 + 8); SCHED(); }
      f32x4 n0[2][4], n1[2][4]; float rc[2][4];
#pragma unroll
      for (int i = 0; i < 8; i++) { n1[i >> 2][i & 3] = mfma16(cat(e1[i & 3][0], e1[i & 3][1]), bq[i >> 2][i & 3], eps); SCHED(); }
#pragma unroll
      for (int i = 0; i < 8; i++) { n0[i >> 2][i & 3] = mfma16(cat(e0[i & 3][0], e0[i & 3][1]), bq[i >> 2][i & 3], (f32x4){0, 0, 0, 0}); SCHED();
        rc[i >> 2][i & 3] = __builtin_amdgcn_rcpf(n1[i >> 2][i & 3][1]); SCHED(); }
#pragma unroll
      for (int m = 0; m < 2; m++) {
#pragma unroll
        for (int hp_ = 0; hp_ < 2; hp_++) { const f32x4 a = n0[m][2 * hp_] * rc[m][2 * hp_], c2 = n0[m][2 * hp_ + 1] * rc[m][2 * hp_ + 1];
          gst128c(rws, o16[m] + hp_ * 1024, (u32x4){pk(a[0], a[1]), pk(a[2], a[3]), pk(c2[0], c2[1]), pk(c2[2], c2[3])}); SCHED(); }
        gst64c(rws, o8, m * 20480, (u32x2){pk(mul1(n1[m][0][0], rc[m][0]), mul1(n1[m][1][0], rc[m][1])), pk(mul1(n1[m][2][0], rc[m][2]), mul1(n1[m][3][0], rc[m][3]))}); SCHED();
      }
      ST(12);
      BAR();
      ST(4);
    } else {
      const int rg = lw >> 1, c = lw & 1;
      unsigned aA[2]; { const int row = rg * 64 + r16;
#pragma unroll
        for (int kk = 0; kk < 2; kk++) aA[kk] = L + row * 128 + (((4 * kk + g) ^ ((row >> 1) & 7)) << 4); }
      unsigned aB[5];
#pragma unroll
      for (int n = 0; n < 5; n++) { int sec, off; if (n < 4) { sec = n >> 1; off = dl + 80 * c + 40 * (n & 1) + 8 * p4; } else { sec = p4 >> 1; off = dl + 80 * c + 40 * (p4 & 1) + 32; }
        off ^= ((g & 1) << 5) ^ (sec << 4); aB[n] = L + XB + sec * WB + (8 * g + q4) * 192 + off; }
      prep_wo();
      asm volatile("" : "+v"(vo[0]), "+v"(vo[1]), "+v"(vo[2]));
      f32x4 acc[4][5];
      u32x4 fa[2][4]; u32x2 fb[2][5][2];
      const float i4 = r16 < 8 ? 1.f : 0.f;
#pragma unroll
      for (int m = 0; m < 4; m++)
#pragma unroll
        for (int n = 0; n < 5; n++) { const float iv = n < 2 ? 1.f : n < 4 ? 0.f : i4; acc[m][n] = (f32x4){iv, iv, iv, iv}; PIN(acc[m][n]); }
      unsigned aA2[2], aB2[5];
#pragma unroll
      for (int i = 0; i < 2; i++) { aA2[i] = aA[i] + 2 * SLOT; PIN(aA[i]); PIN(aA2[i]); }
#pragma unroll
      for (int i = 0; i < 5; i++) { aB2[i] = aB[i] + 2 * SLOT; PIN(aB[i]); PIN(aB2[i]); }
      auto rd = [&](int t, int slot, int kk) {
        const int o = slot == 1 ? SLOT : 0;
        if (t < 4) fa[kk][t] = ld128(slot == 2 ? aA2[kk] : aA[kk], o + t * 2048);
        else { const int n2 = (t - 4) >> 1, h = (t - 4) & 1; fb[kk][n2][h] = ldtr(slot == 2 ? aB2[n2] : aB[n2], o + kk * 32 * 192 + h * 768); } };
      auto half = [&](int slot, int kk) {
#pragma unroll
        for (int t = 0; t < 20; t++) {
          const u32x4 w = cat(fb[1 - kk][t >> 2][0], fb[1 - kk][t >> 2][1]);
          MFMA_ACC(acc[t & 3][t >> 2], fa[1 - kk][t & 3], w);
          SCHED();
          if (t < 14) rd(t, slot, kk);
          if (t == 18) WAITL0();
          SCHED();
        }
      };
      __builtin_amdgcn_s_setprio(2);
      WAITL0(); BAR(); ST(14);
#pragma unroll
      for (int t = 0; t < 14; t++) { rd(t, 0, 0); SCHED(); }
      WAITL0(); SCHED();
      half(0, 1);
#pragma unroll 1
      for (int s = 0; s < 3; s++) {
        BAR(); SCHED(); half(1, 0); half(1, 1);
        BAR(); SCHED(); half(2, 0); half(2, 1);
        BAR(); if (s == 0) ST(15); SCHED(); half(0, 0); half(0, 1);
        if (s == 0 && wv == 7) { WAITV(0); if (lane == 0) set_flag(ws, FL_G + rb * 8 + cg, tag); }
      }
      BAR(); SCHED();
      const unsigned fidx = FL_G + r16 * 8 + cg;
      unsigned fv = 0;
      if (wv == 7) fv = __hip_atomic_load((GAS unsigned*)(ws + WS_FLAG + fidx * 128), __ATOMIC_RELAXED, __HIP_MEMORY_SCOPE_AGENT);
      ST(1);
#pragma unroll
      for (int nn = 0; nn < 5; nn++) { const int n = nn < 2 ? nn : nn == 2 ? 4 : nn - 1;
#pragma unroll
        for (int m = 0; m < 4; m++) acc[m][n] = mfma16(fa[1][m], cat(fb[1][n][0], fb[1][n][1]), acc[m][n]); }
      f32x4 R[7];
#pragma unroll
      for (int i = 0; i < 7; i++) R[i] = (f32x4){0, 0, 0, 0};
#pragma unroll
      for (int ks = 0; ks < 2; ks++) {
        u32x4 T[6];
#pragma unroll
        for (int hf = 0; hf < 2; hf++) { const int m = 2 * ks + hf;
#pragma unroll
          for (int e = 0; e < 2; e++) {
            T[0][2 * hf + e] = phi2(acc[m][0][2 * e], acc[m][0][2 * e + 1]);
            T[1][2 * hf + e] = phi2(acc[m][1][2 * e], acc[m][1][2 * e + 1]);
            T[2][2 * hf + e] = pk(acc[m][2][2 * e], acc[m][2][2 * e + 1]);
            T[3][2 * hf + e] = pk(acc[m][3][2 * e], acc[m][3][2 * e + 1]);
            T[4][2 * hf + e] = phi2(acc[m][4][2 * e], acc[m][4][2 * e + 1]);
            T[5][2 * hf + e] = r16 < 8 ? 0x3f803f80u : pk(acc[m][4][2 * e], acc[m][4][2 * e + 1]);
          } }
        R[0] = mfma16(T[0], T[2], R[0]); R[1] = mfma16(T[1], T[3], R[1]);
        R[2] = mfma16(T[0], T[5], R[2]); R[3] = mfma16(T[1], T[5], R[3]);
        R[4] = mfma16(T[4], T[2], R[4]); R[5] = mfma16(T[4], T[3], R[5]); R[6] = mfma16(T[4], T[5], R[6]);
      }
      f32x4 M23, M456;
#pragma unroll
      for (int i = 0; i < 4; i++) {
        M23[i] = (q4 & 1) ? R[3][i] : R[2][i];
        const float r45 = (g == 0) ? R[4][i] : R[5][i];
        M456[i] = __uint_as_float(__builtin_amdgcn_permlane32_swap(__float_as_uint(r45), __float_as_uint(R[6][i]), false, false)[0]);
      }
      f32x4 k0, k1;
      const unsigned aR = L + G_RED + lw * 2048 + lane * 16, aR2 = L + G_RED + (lw ^ 2) * 2048 + lane * 16;
      if (rg) { k0 = M23; k1 = M456; st128(aR, 0, __builtin_bit_cast(u32x4, R[0])); st128(aR, 1024, __builtin_bit_cast(u32x4, R[1])); }
      else { k0 = R[0]; k1 = R[1]; st128(aR, 0, __builtin_bit_cast(u32x4, M23)); st128(aR, 1024, __builtin_bit_cast(u32x4, M456)); }
      const int irow = c * 2 + rg;
      if (!__all(fv == tag)) wait_flags(ws, fidx, tag);
      ST(8); WAITL0(); BAR(); ST(9);
      { const f32x4 o0 = __builtin_bit_cast(f32x4, ld128(aR2, 0)), o1 = __builtin_bit_cast(f32x4, ld128(aR2, 1024));
#pragma unroll
        for (int i = 0; i < 4; i++) { k0[i] = add1(k0[i], o0[i]); k1[i] = add1(k1[i], o1[i]); } }
      gst128c(rws, lane * 16 + WS_PART + (cg * 16 + rb) * 4096 + irow * 1024, (u32x4){pk(k0[0], k0[1]), pk(k0[2], k0[3]), pk(k1[0], k1[1]), pk(k1[2], k1[3])});
      ST(2);
      load_wo(0, WOA, lw, 4, 10); if (QPRE) load_wo(2, WOB, 3 - lw, 4, 10);
      const unsigned fo = FL_A + (lane < 8 ? rb * 8 + lane : rt0 + (lane & 3));
      u32x4 sel[2];
#pragma unroll
      for (int o = 0; o < 2; o++)
#pragma unroll
        for (int i = 0; i < 4; i++) sel[o][i] = ((r16 >> 1) == 4 * o + i) ? ((r16 & 1) ? 0x3f800000u : 0x00003f80u) : 0u;
      ST(10);
      u32x4 pv[4][4];
      unsigned pend = 15;
#pragma unroll 1
      for (int it = 0; it < SWEEPS; it++) {
        if (wv == 7) fv = __hip_atomic_load((GAS unsigned*)(ws + WS_FLAG + fo * 128), __ATOMIC_RELAXED, __HIP_MEMORY_SCOPE_AGENT);
#pragma unroll
        for (int rp = 0; rp < 4; rp++) if (pend >> rp & 1)
#pragma unroll
          for (int blk = 0; blk < 4; blk++) pv[blk][rp] = gld128c(rws, g * 4096 + r16 * 16 + blk * 256, WS_PART + (cg * 16 + rp * 4) * 4096 + irow * 1024);
#pragma unroll
        for (int rp = 0; rp < 4; rp++) if ((pend >> rp & 1) && none_empty(mx4(mx4(mx4(mx4(0, pv[0][rp]), pv[1][rp]), pv[2][rp]), pv[3][rp]))) pend &= ~(1u << rp);
        asm volatile("" ::: "memory");
        if (!pend) break;
      }
      ST(3);
#pragma unroll
      for (int bp = 0; bp < 2; bp++) {
        f32x4 sum = {0, 0, 0, 0};
#pragma unroll
        for (int o = 0; o < 2; o++)
#pragma unroll
          for (int rp = 0; rp < 4; rp++) sum = mfma16(sel[o], pv[2 * bp + o][rp], sum);
        st64(L + G_RED + (lw ^ 2) * 2048 + ((g >> 1) * 16 + r16) * 16 + (g & 1) * 8, bp * 512, (u32x2){pk(sum[0], sum[1]), pk(sum[2], sum[3])});
      }
      if (!__all(fv == tag)) wait_flags(ws, fo, tag);
      ST(11); WAITL0(); BAR();
      ST(4);
      __builtin_amdgcn_s_setprio(0);
      if (!QPRE) load_wo(2, WOB, lw, 4, 10);
      WAITV(0);
      BAR();
      load_wo(1, WOC, 3 - lw, 4, 10); load_wo(3, WOD, lw, 4, 10);
      ST(12);
    }
  } else {
    const int ch0 = 320 * hf + 5 * ai;
    const rsrc_t rh = mk(Ah, 640 * 4096);
    if (tid < 480) { const unsigned ar = (tid * 2731) >> 16; st128(L + ar * HCP + (tid - ar * 24) * 16, 0, (u32x4){0, 0, 0, 0}); }
    const bool dia = (g & 1) == (r16 >> 3);
    u32x4 idl, idh, dm, bw[3][2];
#pragma unroll
    for (int i = 0; i < 4; i++) { dm[i] = (dia && ((r16 >> 1) & 3) == i) ? ((r16 & 1) ? 0xffff0000u : 0x0000ffffu) : 0u;
      idl[i] = g < 2 ? dm[i] & 0x3f803f80u : 0u; idh[i] = g < 2 ? 0u : dm[i] & 0x3f803f80u; }
    unsigned aK[5], fK[5];
#pragma unroll
    for (int i = 0; i < 5; i++) { const int row = 8 * q4 + 3 - p4 + (i < 4 ? 4 + 4 * (i >> 1) - 2 * (i & 1) - (g >> 1) : 4 * (g >> 1));
      aK[i] = L + (i < 4 ? A_K : A_V) + row * 640; fK[i] = ((((row >> 2) & 6) | ((row >> 1) & 1)) ^ (g & 1)) << 4; }
    unsigned ko = WS_KVT + hf * 1310720 + (255 - 4 * ai - g) * 5120 + r16 * 16 + wv * 256; PIN(ko);
    const int chl = wv < 4 ? wv : 4;
    unsigned xo[3], xa[3], ha[3], qo[3], oo[3], qa_[3];
#pragma unroll
    for (int u = 0; u < 3; u++) { const unsigned c = tid + 512 * u, blk = (c * 13108) >> 16, ch = c - 5 * blk, row = (c * 1639) >> 16;
      xo[u] = blk * 5120 + ch * 16; xa[u] = L + A_X + ch * XCH + blk * 16; ha[u] = L + (c >> 8) * HCH + 384 + (c & 255) * 16;
      qo[u] = row * 640 + c * 16; oo[u] = row * 1920 + c * 16 + 32 * ai * 2560 + hf * 640; qa_[u] = L + T_SCR + c * 16 + (row >> 3) * 32;
      PIN(xo[u]); PIN(xa[u]); PIN(ha[u]); PIN(qo[u]); PIN(oo[u]); PIN(qa_[u]); }
    unsigned aH = L + chl * HCH + (3 - p4) * HCP + (64 * q4 + 8 * g) * 2;
    unsigned aXr = L + A_X + chl * XCH + (1852 - 1792 - 4 * r16 + 8 * g) * 2;
    unsigned aE = L + A_X + chl * XCH + (2044 - 1792 - 64 * g - 4 * r16) * 2;
    unsigned hpo = (tid & 255) ? ch0 * 4096 + tid * 16 - 8 : 0xffff0000u, yl = (tid >> 2) * 4096 + p4 * 16, ys = L + T_SCR + p4 * (8 * 640 + 32) + (tid >> 2) * 2;
    unsigned aH5 = aH + (4 - chl) * HCH + 128 * lw, aX5 = aXr + (4 - chl) * XCH + 128 * lw, aD = L + A_DMP + lw * 2048 + lane * 16, aF = L + L_DONE + lw * 4;
    prep_wo();
    PIN(aH5); PIN(aX5); PIN(aD); PIN(aF); PIN(aH); PIN(aXr); PIN(aE); PIN(hpo); PIN(yl); PIN(ys); PIN(vo[0]); PIN(vo[1]); PIN(vo[2]);
#pragma unroll
    for (int i = 0; i < 5; i++) { aK[i] += fK[i] ^ (32 * wv); PIN(aK[i]); }
    const float Dc = __uint_as_float((*(const __attribute__((address_space(4))) unsigned*)(AD + ((ch0 + chl) & ~1)) >> (((ch0 + chl) & 1) * 16)) << 16);
    if (tid < 5) *(LAS unsigned*)(size_t)(L + L_DONE + tid * 4) = 0;
    asm volatile("s_waitcnt vmcnt(4)" : "+v"(wk[0]), "+v"(wk[1]), "+v"(wk[2]), "+v"(dm), "+v"(idl), "+v"(idh)::"memory");
#pragma unroll
    for (int u = 0; u < 3; u++)
#pragma unroll
      for (int e = 0; e < 2; e++) { bw[u][e] = dm & __builtin_amdgcn_perm(wk[u][e], wk[u][e], g < 2 ? 0x01000100u : 0x03020302u); asm volatile("" : "+v"(bw[u][e])); }
    u32x4 hv[3]; u32x2 hp[3];
    ST(14);
    if (wv >= P1 && wv < P2) WAITV(2); else WAITV(0);
    ST(15); BAR();
    ST(1);
    auto load_h = [&]() {
#pragma unroll
      for (int u = 0; u < 3; u++) if (u < 2 || wv < 4) {
        hv[u] = gld128(rh, tid * 16, ch0 * 4096 + u * 8192);
        hp[u] = __builtin_amdgcn_raw_buffer_load_b64(rh, hpo, u * 8192, 0); } };
    auto kvt = [&](int nt) {
      u32x4 fr[3][5]; f32x4 k0[3], k1[3], v0[3], v1[3];
#pragma unroll
      for (int u = 0; u < nt; u++)
#pragma unroll
        for (int i = 0; i < 5; i++) { fr[u][i] = ld128(aK[i], 256 * u); SCHED(); }
      if (HPOS == 0) load_h();
      SCHED();
#pragma unroll
      for (int u = 0; u <= nt; u++) {
        if (u < nt) { const f32x4 z = {0, 0, 0, 0};
          k0[u] = mfma16(fr[u][0], bw[u][0], z); SCHED(); k1[u] = mfma16(fr[u][2], bw[u][0], z); SCHED();
          k0[u] = mfma16(fr[u][1], bw[u][1], k0[u]); SCHED(); k1[u] = mfma16(fr[u][3], bw[u][1], k1[u]); SCHED();
          v0[u] = mfma16(fr[u][4], idl, z); SCHED(); v1[u] = mfma16(fr[u][4], idh, z); SCHED(); }
        if (u > 0) { const int w = u - 1;
          gst128c(rws, ko + w * 2048, (u32x4){pk(mul1(k1[w][0], v1[w][0]), mul1(k1[w][1], v1[w][1])), pk(mul1(k1[w][2], v1[w][2]), mul1(k1[w][3], v1[w][3])),
                                              pk(mul1(k0[w][0], v0[w][0]), mul1(k0[w][1], v0[w][1])), pk(mul1(k0[w][2], v0[w][2]), mul1(k0[w][3], v0[w][3]))}); SCHED(); }
      } };
    if (wv < 4) kvt(3); else kvt(2);
    ST(8);
    WAITV(0);
    BAR();
    if (wv == 7 && lane == 0) set_flag(ws, FL_K + hf * 64 + ai, tag);
    ST(2);
    if (HPOS == 1) load_h();
    if (wv >= P2 && wv < P3) warm(wv + 1);
    if (tid < 40) st128(L + A_X + (tid >> 3) * XCH + 4096 + (tid & 7) * 16, 0, (u32x4){0, 0, 0, 0});
    auto hcopy = [&]() {
#pragma unroll
      for (int u = 0; u < 3; u++) if (u < 2 || wv < 4) {
        const unsigned e0 = __builtin_amdgcn_alignbyte(hp[u][1], hp[u][0], 2), e1 = __builtin_amdgcn_alignbyte(hv[u][0], hp[u][1], 2), e2 = __builtin_amdgcn_alignbyte(hv[u][1], hv[u][0], 2),
                       e3 = __builtin_amdgcn_alignbyte(hv[u][2], hv[u][1], 2), e4 = __builtin_amdgcn_alignbyte(hv[u][3], hv[u][2], 2);
        st128(ha[u], 0, hv[u]); st128(ha[u], HCP, (u32x4){e1, e2, e3, e4}); st128(ha[u], 2 * HCP, (u32x4){hp[u][1], hv[u][0], hv[u][1], hv[u][2]}); st128(ha[u], 3 * HCP, (u32x4){e0, e1, e2, e3}); } };
    if (HPOS == 0 && wv != 7) hcopy();
    if (wv == 7) wait_flags(ws, FL_K + hf * 64 + lane, tag);
    ST(9);
    BAR();
    ST(3);
    { u32x4 xv[3];
#pragma unroll
      for (int u = 0; u < 3; u++) if (u < 2 || wv < 4) xv[u] = gld128c(rws, xo[u], WS_KVT + hf * 1310720 + ai * 80);
      SCHED();
      if (HPOS == 1 || wv == 7) hcopy();
      SCHED();
#pragma unroll
      for (int u = 0; u < 3; u++) if (u < 2 || wv < 4) st128(xa[u], 0, xv[u]); }
    if (wv >= P3) warm(wv + 1);
    if (wv == 4) warm(9);
    const rsrc_t rq = mk(Aq, 2048 * 1280);
    u32x4 qv[3];
#pragma unroll
    for (int u = 0; u < 3; u++) if (u < 2 || wv < 4) qv[u] = gld128(rq, qo[u], 32 * ai * 1280 + hf * 640);
    const unsigned yo = WS_YT + (ch0 + chl) * 4096;
    WAITL0(); BAR();
    ST(4);
    auto fin = [&](f32x4 a, u32x2 xr, unsigned off) {
      gst64c(rws, lane * 8 + off, yo, (u32x2){pk(a[0] + Dc * bhi(xr[1]), a[1] + Dc * blo(xr[1])), pk(a[2] + Dc * bhi(xr[0]), a[3] + Dc * blo(xr[0]))}); };
    auto wait_cnt = [&]() {
#pragma unroll 1
      for (int it = 0; it < (1 << 20) && __builtin_amdgcn_readfirstlane(*(volatile LAS unsigned*)(size_t)(L + L_CNT)) < 4; it++) __builtin_amdgcn_s_sleep(1);
      asm volatile("" ::: "memory"); };
    if (wv < 4) {
      f32x4 acc[8], ac5[8];
      u32x4 F[2][8]; u32x2 G[2][8][2];
#pragma unroll
      for (int t = 0; t < 8; t++) { F[0][t] = ld128(aH5, 512 * t); G[0][t][0] = ld64(aX5, 512 * (7 - t)); SCHED(); G[0][t][1] = ld64(aX5, 512 * (7 - t) + 8); SCHED(); }
      auto step = [&](f32x4* ac, int cur, unsigned nh, unsigned nx, int off, int mode) {
        int cnt = 0;
#pragma unroll
        for (int t = 0; t < 8; t++)
#pragma unroll
          for (int p = 0; p <= t; p++) {
            const u32x4 gg = cat(G[cur][t - p][0], G[cur][t - p][1]);
            if (p == 0 && (mode == 1 || mode == 2)) MFMA_NEW(ac[t], F[cur][p], gg); else MFMA_ACC(ac[t], F[cur][p], gg);
            SCHED();
            if (cnt < 24) { const int i = cnt / 3, w = cnt % 3;
              if (w == 0) F[1 - cur][i] = ld128(nh, 512 * i + off); else G[1 - cur][i][w - 1] = ld64(nx, 512 * (7 - i) + off + 8 * (w - 1)); }
            if (mode == 2 && (cnt & 3) == 2 && cnt < 32) { const int d = cnt >> 2; st128(aD, (d < 6 ? WOA - A_DMP + (d >> 1) * 9216 : 0) + (d & 1) * 1024, __builtin_bit_cast(u32x4, ac5[d])); }
            if (mode == 2 && cnt == 32) *(volatile LAS unsigned*)(size_t)aF = 1u;
            if (mode == 3 && cnt == 24) { aH += 128; PIN(aH); }
            if (mode == 3 && cnt == 25) { aXr += 128; PIN(aXr); }
            if (cnt == 34) WAITL0();
            cnt++;
            SCHED();
          }
      };
      step(ac5, 0, aH5, aX5, 64, 1); step(ac5, 1, aH, aXr, 0, 0);
      step(acc, 0, aH, aXr, 64, 2); step(acc, 1, aH, aXr, 128, 3);
#pragma unroll 1
      for (int r = 0; r < 3; r++) { step(acc, 0, aH, aXr, 64, 0); step(acc, 1, aH, aXr, 128, 3); }
      u32x2 xe[8];
#pragma unroll
      for (int t = 0; t < 8; t++) { xe[t] = ld64(aE, 512 * (7 - t)); SCHED(); }
      asm volatile("" : "+v"(acc[0]), "+v"(acc[1]), "+v"(acc[2]), "+v"(acc[3]), "+v"(acc[4]), "+v"(acc[5]), "+v"(acc[6]), "+v"(acc[7]));
      ST(10);
      wait_cnt();
#pragma unroll
      for (int t = 0; t < 8; t++) { fin(acc[t], xe[t], 512 * t); SCHED(); }
    } else {
      __builtin_amdgcn_s_setprio(1);
      { const unsigned r = (lane * 52) >> 8, l = lane - 5 * r, row = 40 * j + 10 * lw + r, bad = lane < 50 ? 0u : 0xf0000000u;
        touch(sw, (row * 3840 + l * 128) | bad, (xcd & 1) * 640, L + L_DUMMY);
        const u32x4 emp = {EMPTY, EMPTY, EMPTY, EMPTY};
        const unsigned c1 = WS_YT + ch0 * 4096 + lw * 5120 + lane * 16, c2 = WS_O + (hf * 64 + ai) * 20480 + lw * 5120 + lane * 16;
#pragma unroll
        for (int i = 0; i < 5; i++) { gst128c(rws, c1 + i * 1024, emp); gst128c(rws, c2 + i * 1024, emp); }
        WAITV(0);
        if (lane == 0) __hip_atomic_fetch_add((LAS unsigned*)(size_t)(L + L_CNT), 1u, __ATOMIC_RELAXED, __HIP_MEMORY_SCOPE_WORKGROUP);
        touch(so_, (row * 1280 + l * 128) | bad, 0, L + L_DUMMY); touch(so_, (row * 1280 + l * 128) | bad, 640, L + L_DUMMY); }
      wait_cnt();
      if (wv == 7 && lane == 0) set_flag(ws, FL_A + hf * 64 + ai, tag);
#pragma unroll 1
      for (int it = 0; it < (1 << 20) && !__all(*(volatile LAS unsigned*)(size_t)(L + L_DONE + p4 * 4) == 1u); it++) __builtin_amdgcn_s_sleep(2);
      asm volatile("" ::: "memory");
      { const unsigned rb_ = L + (lw < 3 ? WOA + lw * 9216 : A_DMP) + lane * 16;
#pragma unroll
        for (int e = 0; e < 2; e++) { f32x4 sm = __builtin_bit_cast(f32x4, ld128(rb_, e * 1024));
#pragma unroll
          for (int w = 1; w < 4; w++) { const f32x4 o = __builtin_bit_cast(f32x4, ld128(rb_, w * 2048 + e * 1024));
#pragma unroll
            for (int i = 0; i < 4; i++) sm[i] = add1(sm[i], o[i]); }
          const u32x2 xr = ld64(aE + 512 * (7 - 2 * lw - e), 0);
          gst64c(rws, lane * 8 + 512 * (2 * lw + e), yo, (u32x2){pk(fma1(Dc, bhi(xr[1]), sm[0]), fma1(Dc, blo(xr[1]), sm[1])), pk(fma1(Dc, bhi(xr[0]), sm[2]), fma1(Dc, blo(xr[0]), sm[3]))}); } }
      load_wo(0, WOA, 3 * lw, 1, lw < 3 ? 3 * lw + 3 : 10);
      if (wv == 7) {
        if (b == 128) {
          wait_flags(ws, lane, tag); wait_flags(ws, 64 + lane, tag); wait_flags(ws, 128 + lane, tag); wait_flags(ws, 192 + lane, tag);
          if (lane == 0) __hip_atomic_store((GAS unsigned*)(ws + WS_EPOCH), tag, __ATOMIC_RELAXED, __HIP_MEMORY_SCOPE_AGENT);
        }
        wait_flags(ws, FL_A + hf * 64 + lane, tag); wait_flags(ws, FL_A + rt0 + p4, tag);
      }
      __builtin_amdgcn_s_setprio(0);
    }
    ST(5);
    WAITL0(); BAR();
    ST(11);
    if (wv >= 4) load_wo(2, WOB, lw, 4, 10); else WAITV(0);
    { u32x4 yv[3];
#pragma unroll 1
      for (int it = 0; it < SWEEPS; it++) {
        unsigned m = 0;
#pragma unroll
        for (int u = 0; u < 3; u++) if (u < 2 || wv < 4) yv[u] = gld128c(rws, yl, WS_YT + (320 * hf + 128 * u) * 4096 + ai * 64);
#pragma unroll
        for (int u = 0; u < 3; u++) if (u < 2 || wv < 4) m = mx4(m, yv[u]);
        asm volatile("" ::: "memory");
        if (none_empty(m)) break;
      }
      ST(6);
      load_wo(wv < 4 ? 1 : 3, wv < 4 ? WOC : WOD, 3 - lw, 4, 10);
#pragma unroll
      for (int u = 0; u < 3; u++) if (u < 2 || wv < 4) {
#pragma unroll
        for (int e = 0; e < 4; e++) { st16(ys, 256 * u + 1280 * e, (u16)yv[u][e]); st16(ys, 256 * u + 1280 * e + 640, (u16)(yv[u][e] >> 16)); } } }
    WAITL0(); BAR();
#pragma unroll
    for (int u = 0; u < 3; u++) if (u < 2 || wv < 4) {
      const u32x4 y = ld128(qa_[u], 0); u32x4 r;
#pragma unroll
      for (int e = 0; e < 4; e++) r[e] = pk(blo(y[e]) * blo(qv[u][e]), bhi(y[e]) * bhi(qv[u][e]));
      __builtin_amdgcn_raw_buffer_store_b128(r, ro, oo[u], 0, OUTAUX); }
    ST(12);
    WAITL0();
  }
  const int kh = wv >> 2, rt = rt0 + lw;
  unsigned aW[3][2];
  { const unsigned base = L + (kh ? WOB : WOA);
#pragma unroll
    for (int e = 0; e < 2; e++) {
#pragma unroll
      for (int par = 0; par < 2; par++) aW[par][e] = base + (4 * g + q4) * 192 + ((dl3 + 32 * e + 8 * p4) ^ (((g + par) & 1) << 5));
      aW[2][e] = base + (20 * q4 + 16 + g) * 192 + ((dl3 + 32 * e + 8 * p4) ^ ((q4 & 1) << 5));
    } }
  u32x2 wf[25][2];
  auto rdo = [&](int i) { const int f = i / 5, n = i % 5, co = (n >> 1) * 64;
    if (f < 4) { wf[i][0] = ldtr(aW[0][n & 1], f * 40 * 192 + co); wf[i][1] = ldtr(aW[1][n & 1], (f * 40 + 20) * 192 + co); }
    else { wf[i][0] = ldtr(aW[2][n & 1], co); wf[i][1] = ldtr(aW[2][n & 1], 80 * 192 + co); } };
  u32x4 of[10];
  auto ld_o = [&](int p, bool full) {
    const unsigned so = WS_O + rt * 20480 + (2 * kh + (p ^ psw)) * 5120;
    if (full)
#pragma unroll
      for (int f = 0; f < 4; f++) of[5 * p + f] = gld128c(rws, lane * 16 + f * 1024, so);
    of[5 * p + 4] = cat(gld64c(rws, lane * 8, so + 4096), gld64c(rws, lane * 8 + 512, so + 4096));
  };
  if (role) { ld_o(0, true); ld_o(1, true); }
  SCHED();
#pragma unroll
  for (int i = 0; i < 25; i++) rdo(i);
  SCHED();
  if (!role) { if (!kh) WAITV(0); ld_o(0, true); ld_o(1, true); }
  { const int dd = kh ? WOD - WOB : WOC - WOA;
#pragma unroll
    for (int e = 0; e < 2; e++) { aW[0][e] += dd; aW[1][e] += dd; aW[2][e] += dd; } }
  f32x4 oa[5];
#pragma unroll
  for (int n = 0; n < 5; n++) { oa[n] = (f32x4){0, 0, 0, 0}; PIN(oa[n]); }
  if (role == 0 && wv < 4) ST(14);
#pragma unroll 1
  for (int it = 0; it < SWEEPS; it++) {
    unsigned m = 0;
#pragma unroll
    for (int i = 0; i < 10; i++) m = mx4(m, of[i]);
    if (none_empty(m)) break;
#pragma unroll 1
    for (; it < SWEEPS && !none_empty(mx4(mx4(0, of[4]), of[9])); it++) { asm volatile("" ::: "memory"); ld_o(0, false); ld_o(1, false); }
    asm volatile("" ::: "memory");
    ld_o(0, true); ld_o(1, true);
  }
  WAITV(0); BAR();
  if (role == 0) ST(5); else ST(14);
  SCHED();
#pragma unroll
  for (int i = 0; i < 50; i++) {
    const u32x4 w = cat(wf[i % 25][0], wf[i % 25][1]);
    MFMA_ACC(oa[i % 5], w, of[i / 5]);
    SCHED();
    if (i < 25) rdo(i);
    SCHED();
  }
  asm volatile("s_nop 7" : "+v"(oa[0]), "+v"(oa[1]), "+v"(oa[2]), "+v"(oa[3]), "+v"(oa[4]));
  if (role == 0) ST(6); else ST(15);
  const unsigned sc = L + T_SCR + lw * 5120;
  if (kh) {
#pragma unroll
    for (int n = 0; n < 5; n++) st128(sc + lane * 16, n * 1024, __builtin_bit_cast(u32x4, oa[n]));
    WAITL0(); BAR();
  } else {
    BAR();
#pragma unroll
    for (int n = 0; n < 5; n++) oa[n] += __builtin_bit_cast(f32x4, ld128(sc + lane * 16, n * 1024));
#pragma unroll
    for (int n = 0; n < 5; n++) st64(sc + r16 * 160 + g * 8, n * 32, (u32x2){pk(oa[n][0], oa[n][1]), pk(oa[n][2], oa[n][3])});
#pragma unroll
    for (int u = 0; u < 3; u++) { const unsigned cc = u * 64 + lane, row = (cc * 205) >> 11;
      if (u < 2 || lane < 32) __builtin_amdgcn_raw_buffer_store_b128(ld128(sc + cc * 16, 0), ro, row * 2400 + cc * 16 + rt * 16 * 2560 + 1280 + cg3 * 160, 0, OUTAUX); }
  }
  ST(7); STEND();
}

extern "C" void kernel_launch(void* const* d_in, const int* in_sizes, int n_in, void* d_out, int out_size, void* d_ws, size_t ws_size, hipStream_t stream) {
  fused_kernel<<<256, 512, 0, stream>>>((const u16*)d_in[0], (const u16*)d_in[1], (const u16*)d_in[2], (const u16*)d_in[3], (const u16*)d_in[4], (const u16*)d_in[5],
                                        (const u16*)d_in[6], (const u16*)d_in[7], (const u16*)d_in[8], (u16*)d_out, (char*)d_ws);
}
```

```cpp
#include <hip/hip_runtime.h>
#include <hip/hip_bf16.h>

typedef unsigned short u16;
typedef unsigned long long u64;
typedef short s16x4 __attribute__((ext_vector_type(4)));
typedef short s16x8 __attribute__((ext_vector_type(8)));
typedef __bf16 bf16x2 __attribute__((ext_vector_type(2)));
typedef __bf16 bf16x8 __attribute__((ext_vector_type(8)));
typedef float f32x2 __attribute__((ext_vector_type(2)));
typedef float f32x4 __attribute__((ext_vector_type(4)));
typedef unsigned u32x2 __attribute__((ext_vector_type(2)));
typedef unsigned u32x4 __attribute__((ext_vector_type(4)));
typedef int i32x4 __attribute__((ext_vector_type(4)));
#define LAS __attribute__((address_space(3)))
#define GAS __attribute__((address_space(1)))
#define DI __device__ __forceinline__
#define WAITV(n) asm volatile("s_waitcnt vmcnt(" #n ")" ::: "memory")
#define WAITL0() __builtin_amdgcn_s_waitcnt(0xC07F)
#define WAITL(n) __builtin_amdgcn_s_waitcnt(0xC07F | ((n) << 8))
#define BAR() do { asm volatile("" ::: "memory"); __builtin_amdgcn_s_barrier(); asm volatile("" ::: "memory"); } while (0)
#define SCHED() __builtin_amdgcn_sched_barrier(0)
#define P1 5
#define WR 15
#define CLD 0
#define WOT 0
#define ROTO 1
#define P0 0
#define CLE 1
#define SPEC 2
#define SD 0
#define CPRIO 0
#define PRE 1
#define LEAP 1
#define OUTAUX 16
#define CODEB 32768
#define STAMP 0

DI f32x4 mfma16(u32x4 a, u32x4 b, f32x4 c) { return __builtin_amdgcn_mfma_f32_16x16x32_bf16(__builtin_bit_cast(bf16x8, a), __builtin_bit_cast(bf16x8, b), c, 0, 0, 0); }
DI u32x4 cat(u32x2 a, u32x2 b) { return __builtin_shufflevector(a, b, 0, 1, 2, 3); }
DI u32x4 ld128(unsigned a, int off) { return *(const LAS u32x4*)((LAS char*)(size_t)a + off); }
DI u32x2 ld64(unsigned a, int off) { return *(const LAS u32x2*)((LAS char*)(size_t)a + off); }
DI u32x2 ldtr(unsigned a, int off) { return __builtin_bit_cast(u32x2, __builtin_amdgcn_ds_read_tr16_b64_v4i16((LAS s16x4*)((LAS char*)(size_t)a + off))); }
DI void st128(unsigned a, int off, u32x4 v) { *(LAS u32x4*)((LAS char*)(size_t)a + off) = v; }
DI void st64(unsigned a, int off, u32x2 v) { *(LAS u32x2*)((LAS char*)(size_t)a + off) = v; }
DI void st16(unsigned a, int off, u16 v) { *(LAS u16*)((LAS char*)(size_t)a + off) = v; }
DI unsigned pk(float lo, float hi) { f32x2 v = {lo, hi}; return __builtin_bit_cast(unsigned, __builtin_convertvector(v, bf16x2)); }
DI float blo(unsigned u) { return __uint_as_float(u << 16); }
DI float bhi(unsigned u) { return __uint_as_float(u & 0xffff0000u); }
DI unsigned phi2(float u0, float u1) {
  const f32x2 u = {u0, u1}, a = u * 1.44269504088896341f - 1.44269504088896341f;
  f32x2 p = {__builtin_amdgcn_fmed3f(u0, __builtin_amdgcn_exp2f(a[0]), 1.f), __builtin_amdgcn_fmed3f(u1, __builtin_amdgcn_exp2f(a[1]), 1.f)};
  p += 1e-6f;
  return pk(p[0], p[1]);
}
DI float add1(float a, float b) { float r = a + b; asm("" : "+v"(r)); return r; }
DI float mul1(float a, float b) { float r = a * b; asm("" : "+v"(r)); return r; }
DI float fma1(float a, float b, float c) { float r = __builtin_fmaf(a, b, c); asm("" : "+v"(r)); return r; }
#define MFMA_NEW(acc, a, b) asm("v_mfma_f32_16x16x32_bf16 %0, %1, %2, 0" : "=&v"(acc) : "v"(a), "v"(b))
#define MFMA_INI(acc, a, b, c) asm("v_mfma_f32_16x16x32_bf16 %0, %1, %2, %3" : "=&v"(acc) : "v"(a), "v"(b), "v"(c))
#define MFMA_ACC(acc, a, b) asm("v_mfma_f32_16x16x32_bf16 %0, %1, %2, %0" : "+v"(acc) : "v"(a), "v"(b))
DI i32x4 make_srd(const void* p, unsigned bytes) { u64 a = (u64)p; i32x4 r; r[0] = (int)(unsigned)a; r[1] = (int)(unsigned)(a >> 32) & 0xffff; r[2] = (int)bytes; r[3] = 0x00020000; return r; }
DI void dma16(i32x4 srd, unsigned voff, unsigned soff, unsigned m0v) {
  asm volatile("s_mov_b32 m0, %3\n\ts_nop 0\n\tbuffer_load_dwordx4 %0, %1, %2 offen lds" ::"v"(voff), "s"(srd), "s"(soff), "s"(m0v) : "memory");
}
DI void dma16x3(i32x4 srd, unsigned v0, unsigned v1, unsigned v2, unsigned soff, unsigned m0v) {
  asm volatile("s_mov_b32 m0, %5\n\ts_nop 0\n\tbuffer_load_dwordx4 %0, %3, %4 offen lds\n\tbuffer_load_dwordx4 %1, %3, %4 offen offset:1024 lds\n\tbuffer_load_dwordx4 %2, %3, %4 offen offset:2048 lds"
               ::"v"(v0), "v"(v1), "v"(v2), "s"(srd), "s"(soff), "s"(m0v) : "memory");
}
DI void touch(i32x4 srd, unsigned voff, unsigned soff, unsigned m0v) {
  asm volatile("s_mov_b32 m0, %3\n\ts_nop 0\n\tbuffer_load_dword %0, %1, %2 offen lds" ::"v"(voff), "s"(srd), "s"(soff), "s"(m0v) : "memory");
}
typedef __amdgpu_buffer_rsrc_t rsrc_t;
DI rsrc_t mk(const void* p, unsigned bytes) { return __builtin_amdgcn_make_buffer_rsrc((void*)p, 0, bytes, 0x00020000); }
constexpr int SC1 = 16;
DI u32x4 gld128(rsrc_t r, unsigned v, unsigned s) { return __builtin_amdgcn_raw_buffer_load_b128(r, v, s, 0); }
DI u32x4 gld128c(rsrc_t r, unsigned v, unsigned s) { return __builtin_amdgcn_raw_buffer_load_b128(r, v, s, SC1); }
DI u32x2 gld64c(rsrc_t r, unsigned v, unsigned s) { return __builtin_amdgcn_raw_buffer_load_b64(r, v, s, SC1); }
DI void gst128c(rsrc_t r, unsigned v, u32x4 d) { __builtin_amdgcn_raw_buffer_store_b128(d, r, v, 0, SC1); }
DI void gst64c(rsrc_t r, unsigned v, unsigned s, u32x2 d) { __builtin_amdgcn_raw_buffer_store_b64(d, r, v, s, SC1); }

constexpr unsigned WS_EPOCH = 0;
constexpr unsigned WS_FLAG = 4096;
constexpr unsigned FL_G = 0;
constexpr unsigned FL_K = 128;
constexpr unsigned FL_A = 256;
#define PRS 4480
#define YS 4480
#define KS 5248
#define OS 20864
constexpr unsigned WS_PART = 1u << 18;
constexpr unsigned WS_O = 1u << 20;
constexpr unsigned WS_KVT = 4u << 20;
constexpr unsigned WS_YT = 8u << 20;
constexpr unsigned WS_DBG = 12u << 20;
constexpr unsigned WS_BYTES = 16u << 20;

DI void set_flag(char* ws, unsigned idx, unsigned tag) { __hip_atomic_store((GAS unsigned*)(ws + WS_FLAG + idx * 128), tag, __ATOMIC_RELAXED, __HIP_MEMORY_SCOPE_AGENT); }
DI void wait_flags(char* ws, unsigned idx, unsigned tag) {
  GAS unsigned* p = (GAS unsigned*)(ws + WS_FLAG + idx * 128);
#pragma unroll 1
  for (int spins = 0; spins < (1 << 18); ++spins) {
    const unsigned v = __hip_atomic_load(p, __ATOMIC_RELAXED, __HIP_MEMORY_SCOPE_AGENT);
    if (__all(v == tag)) break;
    __builtin_amdgcn_s_sleep(1);
  }
  asm volatile("" ::: "memory");
}
DI unsigned ld_flag(char* ws, unsigned idx) { return __hip_atomic_load((GAS unsigned*)(ws + WS_FLAG + idx * 128), __ATOMIC_RELAXED, __HIP_MEMORY_SCOPE_AGENT); }
constexpr unsigned EMPTY = 0xffffffffu;
constexpr int SWEEPS = 1 << 16;
DI unsigned umax3(unsigned a, unsigned b, unsigned c) { return __builtin_elementwise_max(__builtin_elementwise_max(a, b), c); }
DI unsigned mx4(unsigned m, u32x4 v) { return umax3(umax3(m, v[0], v[1]), v[2], v[3]); }
DI bool none_empty(unsigned m) { return __builtin_amdgcn_ballot_w64(m == EMPTY) == 0; }
#define PIN(x) asm volatile("" : "+v"(x))
#define ST(n) SCHED()
#define STEND()

constexpr int SLOT = 40960, XB = 16384, WB = 12288;
constexpr int G_RED = 36864;
constexpr int L_DONE = 161792;
constexpr int L_CNT = 161808;
constexpr int L_DUMMY = 163584;
constexpr int WOP[4] = {131072, 45056, 81920, 0};
constexpr int G_OUT = 112640;
constexpr int HCP = 4640, HCH = 4 * HCP;
constexpr int A_X = 4 * HCH, XCH = 4224;
constexpr int A_H4 = A_X + 5 * XCH;
constexpr int KP = 36 * 256, VP = 32 * 256;
constexpr int A_K = A_X, A_V = A_X + 3 * KP;
constexpr int A_DMP = 113920;
constexpr int T_SCR = 61440;

__global__ void __launch_bounds__(512, 2)
fused_kernel(const u16* __restrict__ Aq, const u16* __restrict__ Ak, const u16* __restrict__ Av, const u16* __restrict__ Ask,
             const u16* __restrict__ Ah, const u16* __restrict__ AD, const u16* __restrict__ Bx, const u16* __restrict__ Bw,
             const u16* __restrict__ Bo, u16* __restrict__ out, char* __restrict__ ws) {
  __shared__ __attribute__((aligned(16))) char smem[163840];
  const int b = blockIdx.x, xcd = b & 7, j = (b >> 3) & 15;
  int role = b >> 7;
  const int tid = threadIdx.x, lane = tid & 63;
  int wv = __builtin_amdgcn_readfirstlane(tid >> 6);
  const unsigned L = (unsigned)(size_t)(LAS char*)smem;
  unsigned stampv = 0; (void)stampv;
  ST(0);
  asm volatile("" ::"s"(Aq), "s"(Ak), "s"(Av), "s"(Ask), "s"(Ah), "s"(AD), "s"(Bx), "s"(Bw), "s"(Bo), "s"(out), "s"(ws));
  const int rb = (xcd >> 1) * 4 + (j >> 2), cg = (xcd & 1) * 4 + (j & 3);
  const int a0 = (160 * cg) & ~63, dl = 160 * cg - a0;
  const int hf = xcd & 1, ai = (xcd >> 1) * 16 + j;
  const i32x4 sx = make_srd(Bx, 2048 * 640 * 2), sw = make_srd(Bw, 640 * 1920 * 2);

  unsigned va[3], vb[3], vx = 0;
  int sp = 0;
  u32x2 wk;
  auto issue_x = [&](int slot) {
#pragma unroll
    for (int u = 0; u < 4; u++) dma16(sx, vx, sp * 128 + u * 32 * 1280, L + slot * SLOT + ((wv & 3) + 4 * u) * 1024);
  };
  auto issue_w = [&](int slot) {
    const int lw = wv & 3; const unsigned lb = L + slot * SLOT, so = (sp * 64 + 16 * lw) * 3840 + 1280 + a0;
    dma16x3(sw, va[0], va[1], va[2], so, lb + XB + lw * 3072);
    dma16x3(sw, vb[0], vb[1], vb[2], so + 1280, lb + XB + WB + lw * 3072);
    sp++;
  };
  auto set_warm = [&]() { const unsigned r = (lane * 26) >> 8, l = lane - r * 10;
    va[0] = (((xcd >> 1) * 512 + 32 * j + lane) * 1280) | (lane < 32 ? 0u : 0xf0000000u);
    va[1] = ((4 * j + r) * 3840 + (l >= 5 ? 640 : 0) + l * 128) | (lane < 40 ? 0u : 0xf0000000u); };
  auto warm = [&](int s) { touch(sx, va[0], s * 128, L + L_DUMMY); touch(sw, va[1], s * 64 * 3840 + 1280 + (xcd & 1) * 640, L + L_DUMMY); };
  const rsrc_t rws = mk(ws, WS_BYTES);
  unsigned tag = 0;
  if (wv == 7) {
    tag = *(const __attribute__((address_space(4))) unsigned*)(ws + WS_EPOCH) + 1u;
    if (role == 0) {
#pragma unroll
      for (int i = 0; i < 4; i++) gst128c(rws, WS_PART + (cg * 16 + rb) * PRS + lane * 16 + i * 1024, (u32x4){EMPTY, EMPTY, EMPTY, EMPTY});
    }
    asm volatile("" : "+s"(tag));
  }
  if (role == 0) {
    if (wv < 4) {
      { const int row = 8 * wv + (lane >> 3); vx = (rb * 128 + row) * 1280 + (((lane & 7) ^ ((row >> 1) & 7)) << 4); }
      ST(13);
      issue_x(0);
#pragma unroll
      for (int u = 0; u < 3; u++) { const unsigned c = u * 64 + lane, row = (c * 171) >> 11, pos = c - row * 12; va[u] = row * 3840 + ((pos ^ ((row >> 3) << 1)) << 4) - 1024 * u; vb[u] = va[u] ^ 16; }
      issue_w(0);
    } else if (wv == 6) {
      u64 pc; asm volatile("s_getpc_b64 %0" : "=s"(pc));
      touch(make_srd((const void*)(pc & ~127ull), CODEB), (lane * 16 + j) * 128, 0, L + L_DUMMY);
    }
  } else {
    const i32x4 sk_ = make_srd(Ak, 2048 * 1280), sv_ = make_srd(Av, 2048 * 1280);
    const unsigned rl = lane >> 4, base = (32 * ai + 4 * wv + rl) * 1280 + hf * 640;
    const unsigned ck = ((lane ^ rl ^ (((wv + 1) & 6) << 1)) & 15) << 4, cv = ((lane ^ rl ^ ((wv & 6) << 1)) & 15) << 4;
    if (wv + P0 <= P1) { set_warm(); warm(wv + P0); }
    asm volatile("global_load_dwordx2 %0, %1, %2" : "=v"(wk) : "v"((320 * hf + 16 * wv + (rl < 2 || (rl == 2 && wv < 4) ? 128 * rl : 0) + (lane & 15)) * 8), "s"(Ask) : "memory");
#pragma unroll
    for (int u = 0; u < 3; u++) {
      dma16(sk_, (base + ck) | (u == 2 ? (ck & 128) << 24 : 0), u * 256, L + A_K + u * KP + (wv + 1) * 1024);
      dma16(sv_, (base + cv) | (u == 2 ? (cv & 128) << 24 : 0), u * 256, L + A_V + u * VP + wv * 1024); }
    if (wv < 3) { const unsigned c0 = ((lane ^ rl) & 15) << 4;
      dma16(sk_, ((32 * ai - 4 + rl) * 1280 + hf * 640 + c0 + wv * 256) | (wv == 2 ? (c0 & 128) << 24 : 0), 0, L + A_K + wv * KP); }
    ST(13);
  }
  asm volatile("" : "+s"(role), "+s"(wv));
  const int lw = wv & 3, g = lane >> 4, r16 = lane & 15, q4 = r16 >> 2, p4 = lane & 3;
  const rsrc_t ro = mk(out, 2048 * 1280 * 2);
  auto wait_cnt = [&](unsigned need = 4) {
#pragma unroll 1
    for (int it = 0; it < (1 << 20) && __builtin_amdgcn_readfirstlane(*(volatile LAS unsigned*)(size_t)(L + L_CNT)) < need; it++) __builtin_amdgcn_s_sleep(1);
    asm volatile("" ::: "memory"); };
  auto arrive = [&]() { if (lane == 0) __hip_atomic_fetch_add((LAS unsigned*)(size_t)(L + L_CNT), 1u, __ATOMIC_RELAXED, __HIP_MEMORY_SCOPE_WORKGROUP); };
  const u32x4 emp = {EMPTY, EMPTY, EMPTY, EMPTY};

  if (role == 0) {
    const int cg3 = cg, rt0 = rb * 8, a3 = a0, dl3 = dl;
    if (wv < 4) {
      auto issue_q = [&](int pos) {
        const unsigned so = ((sp - 10) * 64 + 16 * lw) * 3840 + a0;
        dma16x3(sw, va[0], va[1], va[2], so, L + pos + lw * 3072);
        sp++;
      };
      sp = 1;
      PIN(vb[0]); PIN(vb[1]); PIN(vb[2]);
      issue_x(1); issue_w(1);
      ST(15);
      unsigned aX[2]; { const int row = lw * 32 + r16;
#pragma unroll
        for (int kk = 0; kk < 2; kk++) aX[kk] = L + row * 128 + (((4 * kk + g) ^ ((row >> 1) & 7)) << 4); }
      u32x4 xq[10][2][2];
      constexpr int QP[10] = {45056, 45056 + WB, 45056 + 2 * WB, 81920, 81920 + WB, 81920 + 2 * WB, 81920 + 3 * WB, 0, WB, 2 * WB};
#pragma unroll
      for (int s = 0; s < 10; s++) {
        if (s < 9) WAITV(10); else WAITV(9);
        WAITL0(); BAR();
        if (s < 8) issue_x((s + 2) % 3); else issue_q(QP[s == 8 ? 0 : 3]);
#pragma unroll
        for (int kk = 0; kk < 2; kk++)
#pragma unroll
          for (int m = 0; m < 2; m++) xq[s][kk][m] = ld128(aX[kk] + (s % 3) * SLOT, m * 2048);
        if (s < 8) issue_w((s + 2) % 3);
        else if (s == 8) { issue_q(QP[1]); issue_q(QP[2]); }
        else { issue_q(QP[4]); issue_q(QP[5]); }
      }
      WAITL0(); BAR();
      issue_q(QP[6]); issue_q(QP[7]); issue_q(QP[8]); issue_q(QP[9]);
      unsigned aQ[5];
#pragma unroll
      for (int n = 0; n < 5; n++) { const int off = (n < 4 ? dl + 40 * n + 8 * p4 : dl + 40 * p4 + 32) ^ ((g & 1) << 5); aQ[n] = L + QP[0] + (8 * g + q4) * 192 + off; }
      unsigned aE[4]; { const int col = p4 == 0 ? 8 + q4 : 0; aE[0] = L + G_RED + lane * 16; aE[1] = L + G_RED + r16 * 16; aE[2] = L + G_RED + (16 * g + col) * 16; aE[3] = L + G_RED + col * 16; }
      PIN(aE[0]); PIN(aE[1]); PIN(aE[2]); PIN(aE[3]);
      f32x4 qa[2][5];
#pragma unroll
      for (int m = 0; m < 2; m++)
#pragma unroll
        for (int n = 0; n < 5; n++) { qa[m][n] = (f32x4){1.f, 1.f, 1.f, 1.f}; asm volatile("" : "+v"(qa[m][n])); }
      ST(1);
      {
        u32x2 fw[2][5][2];
        auto rdw = [&](int ks, int n, int h) {
          if (h == 0 && ks == 6) { aQ[n] += QP[3] - QP[0]; asm volatile("" : "+v"(aQ[n])); }
          if (h == 0 && ks == 14) { aQ[n] -= QP[3]; asm volatile("" : "+v"(aQ[n])); }
          fw[ks & 1][n][h] = ldtr(aQ[n], (ks - (ks < 6 ? 0 : ks < 14 ? 6 : 14)) * 32 * 192 + 768 * h); };
        unsigned aP = L + (lane ? L_DUMMY + lane * 4 : L_DONE); PIN(aP);
        ST(8); WAITV(0); BAR(); ST(10);
#pragma unroll
        for (int n = 0; n < 5; n++) { rdw(0, n, 0); rdw(0, n, 1); }
        SCHED();
#pragma unroll
        for (int ks = 0; ks < 20; ks++) {
          if (ks == 5 || ks == 13 || ks == 19) { asm volatile("ds_add_u32 %0, %1" ::"v"(aP), "v"(1u << (ks == 5 ? 0 : ks == 13 ? 8 : 16)) : "memory"); SCHED(); }
#pragma unroll
          for (int n = 0; n < 5; n++) {
            if (n == 0) WAITL(4); else if (n == 3) WAITL(6);
            SCHED();
            const u32x4 w = cat(fw[ks & 1][n][0], fw[ks & 1][n][1]);
            MFMA_ACC(qa[0][n], w, xq[ks >> 1][ks & 1][0]);
            SCHED();
            if (ks < 19) rdw(ks + 1, n, 0);
            SCHED();
            MFMA_ACC(qa[1][n], w, xq[ks >> 1][ks & 1][1]);
            SCHED();
            if (ks < 19) rdw(ks + 1, n, 1);
            SCHED();
          }
        }
        asm volatile("s_nop 7" : "+v"(qa[0][4]), "+v"(qa[1][4]), "+v"(qa[0][3]), "+v"(qa[1][3]), "+v"(qa[0][2]), "+v"(qa[1][2]), "+v"(qa[0][1]), "+v"(qa[1][1]), "+v"(qa[0][0]), "+v"(qa[1][0]));
      }
      ST(11);
      u32x4 bq[2][4];
#pragma unroll
      for (int m = 0; m < 2; m++) {
        const unsigned t0 = phi2(qa[m][4][0], qa[m][4][1]), t1 = phi2(qa[m][4][2], qa[m][4][3]);
#pragma unroll
        for (int h = 0; h < 4; h++) { bq[m][h] = (u32x4){phi2(qa[m][h][0], qa[m][h][1]), phi2(qa[m][h][2], qa[m][h][3]), g == h ? t0 : 0u, g == h ? t1 : 0u}; PIN(bq[m][h]); } }
      const unsigned ob = WS_O + (rb * 8 + lw * 2) * OS + (cg >> 1) * 5120;
      unsigned o16[2] = {ob + (cg & 1) * 2048 + lane * 16, ob + (cg & 1) * 2048 + lane * 16 + OS}, o8 = ob + 4096 + (cg & 1) * 8 + lane * 16;
      f32x4 eps = {0, 1e-6f, 0, 0};
      unsigned aE4 = aE[2] ^ 32, aE5 = aE[3] ^ 32;
      PIN(o16[0]); PIN(o16[1]); PIN(o8); PIN(eps); PIN(aE4); PIN(aE5);
      if (lane == 0) __hip_atomic_fetch_add((LAS unsigned*)(size_t)(L + L_DONE), 1u << 24, __ATOMIC_RELAXED, __HIP_MEMORY_SCOPE_WORKGROUP);
      ST(2);
      SCHED(); WAITL0(); BAR();
      ST(3);
      u32x2 e0[4][2], e1[4][2];
#pragma unroll
      for (int h = 0; h < 4; h++) { e1[h][0] = ld64(h < 2 ? aE[2] : aE4, (h >> 1) * 2048 + (h & 1) * 64); SCHED(); e1[h][1] = ld64(h < 2 ? aE[3] : aE5, (h >> 1) * 2048 + (h & 1) * 320 + 520); SCHED(); }
#pragma unroll
      for (int h = 0; h < 4; h++) { e0[h][0] = ld64(aE[0], 4096 + (h >> 1) * 2048 + 8 * (h & 1)); SCHED(); e0[h][1] = ld64(aE[1], (h >> 1) * 2048 + (h & 1) * 256 + 8); SCHED(); }
      f32x4 n0[2][4], n1[2][4]; float rc[2][4];
#pragma unroll
      for (int i = 0; i < 8; i++) { n1[i >> 2][i & 3] = mfma16(cat(e1[i & 3][0], e1[i & 3][1]), bq[i >> 2][i & 3], eps); SCHED(); }
#pragma unroll
      for (int i = 0; i < 8; i++) { n0[i >> 2][i & 3] = mfma16(cat(e0[i & 3][0], e0[i & 3][1]), bq[i >> 2][i & 3], (f32x4){0, 0, 0, 0}); SCHED();
        rc[i >> 2][i & 3] = __builtin_amdgcn_rcpf(n1[i >> 2][i & 3][1]); SCHED(); }
#pragma unroll
      for (int m = 0; m < 2; m++) {
#pragma unroll
        for (int hp_ = 0; hp_ < 2; hp_++) { const f32x4 a = n0[m][2 * hp_] * rc[m][2 * hp_], c2 = n0[m][2 * hp_ + 1] * rc[m][2 * hp_ + 1];
          gst128c(rws, o16[m] + hp_ * 1024, (u32x4){pk(a[0], a[1]), pk(a[2], a[3]), pk(c2[0], c2[1]), pk(c2[2], c2[3])}); SCHED(); }
        gst64c(rws, o8, m * OS, (u32x2){pk(mul1(n1[m][0][0], rc[m][0]), mul1(n1[m][1][0], rc[m][1])), pk(mul1(n1[m][2][0], rc[m][2]), mul1(n1[m][3][0], rc[m][3]))}); SCHED();
      }
      ST(12);
      if (SPEC) { if (SPEC == 1) WAITV(0); arrive(); }
    } else {
      const int rg = lw >> 1, c = lw & 1;
      unsigned aA[2]; { const int row = rg * 64 + r16;
#pragma unroll
        for (int kk = 0; kk < 2; kk++) aA[kk] = L + row * 128 + (((4 * kk + g) ^ ((row >> 1) & 7)) << 4); }
      unsigned aB[5];
#pragma unroll
      for (int n = 0; n < 5; n++) { int sec, off; if (n < 4) { sec = n >> 1; off = dl + 80 * c + 40 * (n & 1) + 8 * p4; } else { sec = p4 >> 1; off = dl + 80 * c + 40 * (p4 & 1) + 32; }
        off ^= ((g & 1) << 5) ^ (sec << 4); aB[n] = L + XB + sec * WB + (8 * g + q4) * 192 + off; }
      if (lane < 5) *(LAS unsigned*)(size_t)(L + L_DONE + lane * 4) = 0;
      f32x4 acc[4][5];
      u32x4 fa[2][4]; u32x2 fb[2][5][2];
      const float i4 = r16 < 8 ? 1.f : 0.f;
      f32x4 c1 = {1.f, 1.f, 1.f, 1.f}, c4 = {i4, i4, i4, i4}; PIN(c1); PIN(c4);
      unsigned aA2[2], aB2[5];
#pragma unroll
      for (int i = 0; i < 2; i++) { aA2[i] = aA[i] + 2 * SLOT; PIN(aA[i]); PIN(aA2[i]); }
#pragma unroll
      for (int i = 0; i < 5; i++) { aB2[i] = aB[i] + 2 * SLOT; PIN(aB[i]); PIN(aB2[i]); }
      auto rd = [&](int t, int slot, int kk) {
        const int o = slot == 1 ? SLOT : 0;
        if (t < 4) fa[kk][t] = ld128(slot == 2 ? aA2[kk] : aA[kk], o + t * 2048);
        else { const int n2 = (t - 4) >> 1, h = (t - 4) & 1; fb[kk][n2][h] = ldtr(slot == 2 ? aB2[n2] : aB[n2], o + kk * 32 * 192 + h * 768); } };
      auto half = [&](int slot, int kk, bool first = false) {
#pragma unroll
        for (int t = 0; t < 20; t++) {
          const u32x4 w = cat(fb[1 - kk][t >> 2][0], fb[1 - kk][t >> 2][1]);
          if (!first) MFMA_ACC(acc[t & 3][t >> 2], fa[1 - kk][t & 3], w);
          else if (t < 8) MFMA_INI(acc[t & 3][t >> 2], fa[1 - kk][t & 3], w, c1);
          else if (t < 16) MFMA_NEW(acc[t & 3][t >> 2], fa[1 - kk][t & 3], w);
          else MFMA_INI(acc[t & 3][t >> 2], fa[1 - kk][t & 3], w, c4);
          SCHED();
          if (t < 14) rd(t, slot, kk);
          if (t == 18) WAITL0();
          SCHED();
        }
      };
      unsigned fv = 0;
      __builtin_amdgcn_s_setprio(2);
      WAITL0(); BAR(); ST(14);
#pragma unroll
      for (int t = 0; t < 14; t++) { rd(t, 0, 0); SCHED(); }
      WAITL0(); SCHED();
      half(0, 1, true);
#pragma unroll 1
      for (int s = 0; s < 3; s++) {
        BAR(); SCHED(); half(1, 0); half(1, 1);
        BAR(); SCHED(); half(2, 0); half(2, 1);
        BAR(); if (s == 0) ST(15); SCHED(); half(0, 0); half(0, 1);
        if (s == 0 && wv == 7) { WAITV(0); if (lane == 0) set_flag(ws, FL_G + rb * 8 + cg, tag); }
      }
      BAR(); SCHED();
      const int irow = c * 2 + rg;
      ST(1);
#pragma unroll
      for (int nn = 0; nn < 5; nn++) { const int n = nn < 2 ? nn : nn == 2 ? 4 : nn - 1;
#pragma unroll
        for (int m = 0; m < 4; m++) acc[m][n] = mfma16(fa[1][m], cat(fb[1][n][0], fb[1][n][1]), acc[m][n]); }
      f32x4 R[7];
#pragma unroll
      for (int i = 0; i < 7; i++) R[i] = (f32x4){0, 0, 0, 0};
#pragma unroll
      for (int ks = 0; ks < 2; ks++) {
        u32x4 T[6];
#pragma unroll
        for (int hf = 0; hf < 2; hf++) { const int m = 2 * ks + hf;
#pragma unroll
          for (int e = 0; e < 2; e++) {
            T[0][2 * hf + e] = phi2(acc[m][0][2 * e], acc[m][0][2 * e + 1]);
            T[1][2 * hf + e] = phi2(acc[m][1][2 * e], acc[m][1][2 * e + 1]);
            T[2][2 * hf + e] = pk(acc[m][2][2 * e], acc[m][2][2 * e + 1]);
            T[3][2 * hf + e] = pk(acc[m][3][2 * e], acc[m][3][2 * e + 1]);
            T[4][2 * hf + e] = phi2(acc[m][4][2 * e], acc[m][4][2 * e + 1]);
            T[5][2 * hf + e] = r16 < 8 ? 0x3f803f80u : pk(acc[m][4][2 * e], acc[m][4][2 * e + 1]);
          } }
        R[0] = mfma16(T[0], T[2], R[0]); R[1] = mfma16(T[1], T[3], R[1]);
        R[2] = mfma16(T[0], T[5], R[2]); R[3] = mfma16(T[1], T[5], R[3]);
        R[4] = mfma16(T[4], T[2], R[4]); R[5] = mfma16(T[4], T[3], R[5]); R[6] = mfma16(T[4], T[5], R[6]);
        if (ks == 0) { SCHED(); if (wv == 7) fv = ld_flag(ws, FL_G + r16 * 8 + cg); SCHED(); }
      }
      f32x4 M23, M456;
#pragma unroll
      for (int i = 0; i < 4; i++) {
        M23[i] = (q4 & 1) ? R[3][i] : R[2][i];
        const float r45 = (g == 0) ? R[4][i] : R[5][i];
        M456[i] = __uint_as_float(__builtin_amdgcn_permlane32_swap(__float_as_uint(r45), __float_as_uint(R[6][i]), false, false)[0]);
      }
      f32x4 k0, k1;
      const unsigned aR = L + G_RED + lw * 2048 + lane * 16, aR2 = L + G_RED + (lw ^ 2) * 2048 + lane * 16;
      if (rg) { k0 = M23; k1 = M456; st128(aR, 0, __builtin_bit_cast(u32x4, R[0])); st128(aR, 1024, __builtin_bit_cast(u32x4, R[1])); }
      else { k0 = R[0]; k1 = R[1]; st128(aR, 0, __builtin_bit_cast(u32x4, M23)); st128(aR, 1024, __builtin_bit_cast(u32x4, M456)); }
      if (!__all(fv == tag)) wait_flags(ws, FL_G + r16 * 8 + cg, tag);
      ST(8); WAITL0(); BAR(); ST(9);
      { const f32x4 o0 = __builtin_bit_cast(f32x4, ld128(aR2, 0)), o1 = __builtin_bit_cast(f32x4, ld128(aR2, 1024));
#pragma unroll
        for (int i = 0; i < 4; i++) { k0[i] = add1(k0[i], o0[i]); k1[i] = add1(k1[i], o1[i]); } }
      gst128c(rws, lane * 16 + WS_PART + (cg * 16 + rb) * PRS + irow * 1024, (u32x4){pk(k0[0], k0[1]), pk(k0[2], k0[3]), pk(k1[0], k1[1]), pk(k1[2], k1[3])});
      ST(2);
      if (CPRIO == 0) __builtin_amdgcn_s_setprio(0);
      ST(10);
      u32x4 pv[4][4];
      const i32x4 so_ = make_srd(Bo, 640 * 640 * 2);
      unsigned vo[3];
#pragma unroll
      for (int u = 0; u < 3; u++) { const unsigned cc = u * 64 + lane, row = (cc * 171) >> 11, ps = cc - row * 12; vo[u] = row * 1280 + ((ps ^ (((row >> 2) & 1) << 1)) << 4) - 1024 * u; PIN(vo[u]); }
      int gi = lw;
      const unsigned fo = FL_A + rt0 + (lane & 7);
      auto prog = [&]() { return (unsigned)__builtin_amdgcn_readfirstlane(*(volatile LAS unsigned*)(size_t)(L + L_DONE)); };
      auto level = [&]() { return __builtin_popcount(prog() & 0x04040404u); };
      auto wo_groups = [&](int n, int lv) {
#pragma unroll 1
        for (; n > 0 && gi < 40 && gi < 10 * lv + 10; n--, gi += 4) { const int pos = (gi < 10 ? WOP[0] : gi < 20 ? WOP[1] - 30720 : gi < 30 ? WOP[2] - 61440 : WOP[3] - 92160) + gi * 3072;
          dma16x3(so_, vo[0], vo[1], vo[2], gi * 16 * 1280 + a3, L + pos); } };
      wo_groups(10, 0);
      u32x4 sel[2];
#pragma unroll
      for (int o = 0; o < 2; o++)
#pragma unroll
        for (int i = 0; i < 4; i++) sel[o][i] = ((r16 >> 1) == 4 * o + i) ? ((r16 & 1) ? 0x3f800000u : 0x00003f80u) : 0u;
      const unsigned pb = WS_PART + cg * 16 * PRS + irow * 1024, gr = (rb >> 2) ^ 3, pvo = g * PRS + r16 * 16;
      const i32x4 sws = make_srd(ws, WS_BYTES);
      if (PRE) {
#pragma unroll 1
        for (int it = 0; it < (1 << 20) && !level(); it++) __builtin_amdgcn_s_sleep(1);
        asm volatile("" ::: "memory");
        wo_groups(PRE == 1 ? 10 : 0, 1); }
      ST(13);
      f32x4 sum[2];
      auto reduce = [&]() {
#pragma unroll
        for (int i = 0; i < 8; i++)
#pragma unroll
          for (int bp = 0; bp < 2; bp++) { sum[bp] = mfma16(sel[i & 1], pv[i >> 1][2 * bp + (i & 1)], i ? sum[bp] : (f32x4){0, 0, 0, 0}); SCHED(); } };
#pragma unroll
      for (int i = 0; i < 16; i++) { pv[i >> 2][i & 3] = gld128c(rws, pvo + (i & 3) * 256, pb + (gr ^ (i >> 2)) * 4 * PRS); SCHED(); }
      if (wv == 7) fv = ld_flag(ws, fo);
      SCHED();
      reduce();
      const bool oset = __all(fv == tag);
      SCHED();
      wo_groups(3, level());
      if (__builtin_amdgcn_ballot_w64(__builtin_isunordered(sum[0][0], sum[1][0]))) {
      unsigned pend = 15;
#pragma unroll 1
      for (int it = 0; it < SWEEPS; it++) {
#pragma unroll
        for (int rp = 0; rp < 4; rp++) if ((pend >> rp & 1) && none_empty(mx4(mx4(mx4(mx4(0, pv[rp][0]), pv[rp][1]), pv[rp][2]), pv[rp][3]))) pend &= ~(1u << rp);
        if (!pend) break;
#define GRP(r, s, a, b, c, d) "s_bitcmp1_b32 %18, " #r "\n\ts_cbranch_scc0 " #r "f\n\tbuffer_load_dwordx4 %" #a ", %16, %17, %" #s " offen sc1\n\tbuffer_load_dwordx4 %" #b ", %16, %17, %" #s " offen offset:256 sc1\n\t" \
                              "buffer_load_dwordx4 %" #c ", %16, %17, %" #s " offen offset:512 sc1\n\tbuffer_load_dwordx4 %" #d ", %16, %17, %" #s " offen offset:768 sc1\n" #r ":\n\t"
        asm volatile("s_nop 4\n\t" GRP(0, 19, 0, 1, 2, 3) GRP(1, 20, 4, 5, 6, 7) GRP(2, 21, 8, 9, 10, 11) GRP(3, 22, 12, 13, 14, 15) "s_waitcnt vmcnt(0)"
                     : "+v"(pv[0][0]), "+v"(pv[0][1]), "+v"(pv[0][2]), "+v"(pv[0][3]), "+v"(pv[1][0]), "+v"(pv[1][1]), "+v"(pv[1][2]), "+v"(pv[1][3]),
                       "+v"(pv[2][0]), "+v"(pv[2][1]), "+v"(pv[2][2]), "+v"(pv[2][3]), "+v"(pv[3][0]), "+v"(pv[3][1]), "+v"(pv[3][2]), "+v"(pv[3][3])
                     : "v"(pvo), "s"(sws), "s"(pend), "s"(pb + gr * 4 * PRS), "s"(pb + (gr ^ 1) * 4 * PRS), "s"(pb + (gr ^ 2) * 4 * PRS), "s"(pb + (gr ^ 3) * 4 * PRS) : "scc", "memory");
#undef GRP
      }
      reduce();
      }
      ST(3);
      {
#pragma unroll
        for (int bp = 0; bp < 2; bp++) st64(L + G_RED + (lw ^ 2) * 2048 + ((g >> 1) * 16 + r16) * 16 + (g & 1) * 8, bp * 512, (u32x2){pk(sum[bp][0], sum[bp][1]), pk(sum[bp][2], sum[bp][3])}); }
      if (!oset) wait_flags(ws, fo, tag);
      ST(11);
#pragma unroll 1
      for (int it = 0; it < (1 << 20) && gi < 40; it++) { const int lv = level(); if (lv == 4) break; if (gi < 10 * lv + 10) wo_groups(1, lv); else __builtin_amdgcn_s_sleep(2); }
      WAITL0(); BAR();
      ST(4);
      __builtin_amdgcn_s_setprio(0);
      wo_groups(10, 3);
      ST(12);
      unsigned aW[3][2];
#pragma unroll
      for (int e = 0; e < 2; e++) {
#pragma unroll
        for (int par = 0; par < 2; par++) aW[par][e] = L + (4 * g + q4) * 192 + ((dl3 + 32 * e + 8 * p4) ^ (((g + par) & 1) << 5));
        aW[2][e] = L + (20 * q4 + 16 + (g ^ (q4 & 2))) * 192 + ((dl3 + 32 * e + 8 * p4) ^ ((q4 & 1) << 5));
      }
      const unsigned so = WS_O + (rt0 + 2 * lw) * OS, sc = L + (lw < 3 ? G_OUT + lw * 5120 : 30720);
      unsigned lo[2] = {lane * 16u, lane * 16u + OS}, sw_ = sc + r16 * 160 + g * 8, sr = sc + lane * 16, go[5];
#pragma unroll
      for (int u = 0; u < 5; u++) { const unsigned c = lane + 64 * u, row = (c * 6554) >> 16; go[u] = row * 2400 + c * 16 + (rt0 + 2 * lw) * 16 * 2560 + 1280 + cg3 * 160; PIN(go[u]); }
      PIN(sw_); PIN(sr); PIN(lo[0]); PIN(lo[1]);
      WAITV(0); arrive(); wait_cnt(SPEC ? 8 : 4); if (SD) __builtin_amdgcn_s_sleep(SD);
      f32x4 oa[2][5], ob[2][5];
#pragma unroll 1
      for (int pass = 0; pass < 2; pass++) {
        u32x2 wf[2][5][2]; u32x4 of[6][2];
        auto look = [&]() { asm volatile("" ::: "memory"); return __builtin_amdgcn_raw_buffer_load_b32(rws, ((lane >> 1) & 3) * 5120 + (lane & 1) * 8, so + OS + 4096 + 1008, SC1); };
        unsigned em = 0;
        if (pass == 0) {
          if (!SPEC)
#pragma unroll 1
            for (int it = 0; it < SWEEPS; it++) { em = (unsigned)__builtin_amdgcn_ballot_w64(look() == EMPTY) & 0xff; if (!(em & 0xf) || !(em & 0xf0)) break; }
        } else {
#pragma unroll 1
          for (int it = 0; it < SWEEPS; it++) { unsigned m = 0; asm volatile("" ::: "memory");
#pragma unroll 4
            for (int i = 0; i < 20; i++) m = mx4(mx4(m, gld128c(rws, lo[0], so + i * 1024)), gld128c(rws, lo[1], so + i * 1024));
            if (none_empty(m)) break; }
        }
        asm volatile("" ::: "memory");
        ST(5);
        const int h1 = SPEC ? xcd & 1 : (em & 0xf) ? 1 : (em & 0xf0) ? 0 : (cg3 & 1);
        const unsigned sp_[2] = {so + h1 * 10240, so + (h1 ^ 1) * 10240};
        const int dw[5] = {h1 ? WOP[2] : WOP[0], h1 ? WOP[3] - WOP[2] : WOP[1] - WOP[0], h1 ? WOP[0] - WOP[3] : WOP[2] - WOP[1], h1 ? WOP[1] - WOP[0] : WOP[3] - WOP[2], h1 ? -WOP[1] : -WOP[3]};
        auto rdo = [&](int ks, int n, int h) { const int p = ks / 5, f = ks % 5, co = (n >> 1) * 64, r = f < 4 ? h : 2;
          if (n < 2 && (f == 0 || (f == 4 && h == 0))) { aW[r][n] += dw[p]; PIN(aW[r][n]); }
          wf[ks & 1][n][h] = ldtr(aW[r][n & 1], (f < 4 ? f * 40 + 20 * h : 80 * h) * 192 + co); };
        auto ldo = [&](int ks, int m) { of[ks % 6][m] = gld128c(rws, lo[m] + (ks % 10 & 3) * 1024, sp_[ks / 10] + (ks % 10 >> 2) * 4096); };
#pragma unroll
        for (int n = 0; n < 5; n++) { rdo(0, n, 0); rdo(0, n, 1); }
#pragma unroll 1
        for (int it = 0; it < SWEEPS; it++) {
          unsigned lk = 0;
          if (SPEC) lk = look();
          SCHED();
#pragma unroll
          for (int ks = 0; ks < 5; ks++) { ldo(ks, 0); SCHED(); ldo(ks, 1); SCHED(); }
          if (!SPEC) break;
          em = (unsigned)__builtin_amdgcn_ballot_w64(lk == EMPTY) & 0xff;
          if (!(em >> (4 * h1) & 0xf)) break;
        }
        SCHED();
#pragma unroll
        for (int ks = 0; ks < 20; ks++) {
          if (ks == 5 && em) {
#pragma unroll 1
            for (int it = 0; it < SWEEPS && (__builtin_amdgcn_ballot_w64(look() == EMPTY) & 0xff); it++) {}
            asm volatile("" ::: "memory"); }
#pragma unroll
          for (int n = 0; n < 5; n++) {
            if (n == 0) { if (ks < 16) __builtin_amdgcn_s_waitcnt(0x0478); else if (ks == 16) __builtin_amdgcn_s_waitcnt(0x0476); else if (ks == 17) __builtin_amdgcn_s_waitcnt(0x0474); else if (ks == 18) __builtin_amdgcn_s_waitcnt(0x0472); else __builtin_amdgcn_s_waitcnt(0x0470); }
            else if (n == 3) WAITL(6);
            if (ks < 15 && (n == 1 || n == 4)) ldo(ks + 5, n >> 2);
            SCHED();
            const u32x4 w = cat(wf[ks & 1][n][0], wf[ks & 1][n][1]);
            if (ks % (SPEC ? 20 : 10) == 0) MFMA_NEW((ks ? ob : oa)[0][n], w, of[ks % 6][0]); else MFMA_ACC((!SPEC && ks > 10 ? ob : oa)[0][n], w, of[ks % 6][0]);
            SCHED();
            if (ks < 19) rdo(ks + 1, n, 0);
            SCHED();
            if (ks % (SPEC ? 20 : 10) == 0) MFMA_NEW((ks ? ob : oa)[1][n], w, of[ks % 6][1]); else MFMA_ACC((!SPEC && ks > 10 ? ob : oa)[1][n], w, of[ks % 6][1]);
            SCHED();
            if (ks < 19) rdo(ks + 1, n, 1);
            SCHED();
          }
        }
        if (SPEC) asm volatile("s_nop 7" : "+v"(oa[0][0]), "+v"(oa[0][1]), "+v"(oa[0][2]), "+v"(oa[0][3]), "+v"(oa[0][4]), "+v"(oa[1][0]), "+v"(oa[1][1]), "+v"(oa[1][2]), "+v"(oa[1][3]), "+v"(oa[1][4]));
        else {
          asm volatile("s_nop 7" : "+v"(ob[0][0]), "+v"(ob[0][1]), "+v"(ob[0][2]), "+v"(ob[0][3]), "+v"(ob[0][4]), "+v"(ob[1][0]), "+v"(ob[1][1]), "+v"(ob[1][2]), "+v"(ob[1][3]), "+v"(ob[1][4]));
#pragma unroll
          for (int i = 0; i < 10; i++) oa[i / 5][i % 5] += ob[i / 5][i % 5]; }
        if (__builtin_amdgcn_ballot_w64(__builtin_isunordered(oa[0][0][0], oa[1][0][0])) == 0) break;
#pragma unroll
        for (int i = 0; i < 6; i++) aW[i >> 1][i & 1] += dw[4];
      }
      ST(6);
#pragma unroll
      for (int m = 0; m < 2; m++)
#pragma unroll
        for (int n = 0; n < 5; n++) { st64(sw_, m * 2560 + n * 32, (u32x2){pk(oa[m][n][0], oa[m][n][1]), pk(oa[m][n][2], oa[m][n][3])}); SCHED(); }
      { u32x4 r[5];
#pragma unroll
        for (int u = 0; u < 5; u++) r[u] = ld128(sr, u * 1024);
#pragma unroll
        for (int u = 0; u < 5; u++) __builtin_amdgcn_raw_buffer_store_b128(r[u], ro, go[u], 0, OUTAUX); }
    }
  } else {
    const int ch0 = 320 * hf + 5 * ai;
    const rsrc_t rh = mk(Ah, 640 * 4096);
    u32x4 idl, idh, dm, bw[3][2];
    { const unsigned hm = (g & 1) == (r16 >> 3) ? 0xffffu << ((r16 & 1) * 16) : 0u, one = g < 2 ? 0x3f803f80u : 0u;
#pragma unroll
      for (int i = 0; i < 4; i++) { dm[i] = ((r16 >> 1) & 3) == i ? hm : 0u; idl[i] = dm[i] & one; idh[i] = dm[i] & (one ^ 0x3f803f80u); } }
    unsigned aK[5];
#pragma unroll
    for (int i = 0; i < 5; i++) { const int row = 8 * q4 + 3 - p4 + (i < 4 ? 4 + 4 * (i >> 1) - 2 * (i & 1) - (g >> 1) : 4 * (g >> 1));
      aK[i] = L + (i < 4 ? A_K : A_V) + row * 256 + ((((row >> 1) & 12) ^ (row & 3) ^ (g & 1) ^ (2 * wv)) << 4); PIN(aK[i]); }
    unsigned ko = WS_KVT + hf * 256 * KS + (255 - 4 * ai - g) * KS + r16 * 16 + wv * 256, hpo = (tid & 255) ? ch0 * 4096 + tid * 16 - 8 : 0xffff0000u;
    PIN(ko); PIN(hpo);
    if (wv == 7) { set_warm(); PIN(va[0]); PIN(va[1]); }
    const int chl = wv < 4 ? wv : 4;
    const float Dc = __uint_as_float((*(const __attribute__((address_space(4))) unsigned*)(AD + ((ch0 + chl) & ~1)) >> (((ch0 + chl) & 1) * 16)) << 16);
    if (tid < 6) *(LAS unsigned*)(size_t)(L + L_DONE + tid * 4) = 0;
    asm volatile("s_waitcnt vmcnt(6)" : "+v"(wk), "+v"(dm), "+v"(idl), "+v"(idh)::"memory");
    BAR();
    u32x4 hv[3]; u32x2 hp[3];
    auto load_h = [&]() {
#pragma unroll
      for (int u = 0; u < 3; u++) if (u < 2 || wv < 4) {
        hv[u] = gld128(rh, tid * 16, ch0 * 4096 + u * 8192);
        hp[u] = __builtin_amdgcn_raw_buffer_load_b64(rh, hpo, u * 8192, 0); } };
    auto warm_wo = [&]() { const i32x4 so_ = make_srd(Bo, 640 * 640 * 2);
      const unsigned r = (lane * 52) >> 8, vq = (r * 1280 + (lane - 5 * r) * 128) | (lane < 50 ? 0u : 0xf0000000u);
#pragma unroll
      for (int i = 0; i < 4; i++) touch(so_, vq, (i * 160 + 10 * j) * 1280 + (xcd & 1) * 640, L + L_DUMMY); };
    if (wv == 7) {
#pragma unroll
      for (int s = P1 + 1; s < 10; s++) warm(s);
      const unsigned r = (lane * 52) >> 8, vq = (r * 3840 + (lane - 5 * r) * 128) | (lane < 50 ? 0u : 0xf0000000u);
#pragma unroll
      for (int i = 0; i < 4; i++) touch(sw, vq, (i * 160 + 10 * j) * 3840 + (xcd & 1) * 640, L + L_DUMMY);
    }
#pragma unroll
    for (int u = 0; u < 3; u++)
#pragma unroll
      for (int e = 0; e < 2; e++) { const unsigned w = __builtin_amdgcn_ds_bpermute((16 * u + r16) * 4, wk[e]);
        bw[u][e] = dm & __builtin_amdgcn_perm(w, w, g < 2 ? 0x01000100u : 0x03020302u); PIN(bw[u][e]); }
    ST(14);
    if (wv == 7) asm volatile("s_waitcnt vmcnt(%0)" ::"i"(22 - 2 * P1) : "memory"); else WAITV(0);
    ST(15); BAR();
    ST(1);
    auto kvt = [&](int nt) {
      u32x4 fr[3][5]; f32x4 k0[3], k1[3], v0[3], v1[3];
#pragma unroll
      for (int u = 0; u < nt; u++)
#pragma unroll
        for (int i = 0; i < 5; i++) { fr[u][i] = ld128(aK[i], (i < 4 ? KP : VP) * u); SCHED(); }
      SCHED();
#pragma unroll
      for (int u = 0; u <= nt; u++) {
        if (u < nt) { const f32x4 z = {0, 0, 0, 0};
          k0[u] = mfma16(fr[u][0], bw[u][0], z); SCHED(); k1[u] = mfma16(fr[u][2], bw[u][0], z); SCHED();
          k0[u] = mfma16(fr[u][1], bw[u][1], k0[u]); SCHED(); k1[u] = mfma16(fr[u][3], bw[u][1], k1[u]); SCHED();
          v0[u] = mfma16(fr[u][4], idl, z); SCHED(); v1[u] = mfma16(fr[u][4], idh, z); SCHED(); }
        if (u > 0) { const int w = u - 1;
          gst128c(rws, ko + w * 2048, (u32x4){pk(mul1(k1[w][0], v1[w][0]), mul1(k1[w][1], v1[w][1])), pk(mul1(k1[w][2], v1[w][2]), mul1(k1[w][3], v1[w][3])),
                                              pk(mul1(k0[w][0], v0[w][0]), mul1(k0[w][1], v0[w][1])), pk(mul1(k0[w][2], v0[w][2]), mul1(k0[w][3], v0[w][3]))}); SCHED(); }
      } };
    if (wv < 4) kvt(3); else kvt(2);
    ST(8);
    unsigned xo, xa, ha = L + (tid >> 8) * HCH + 384 + (tid & 255) * 16;
    { const unsigned blk = (tid * 13108) >> 16, ch = tid - 5 * blk; xo = blk * KS + ch * 16; xa = L + A_X + ch * XCH + blk * 16; }
    unsigned aH = L + lw * HCH + (3 - p4) * HCP + (64 * q4 + 8 * g) * 2;
    unsigned aXr = L + A_X + lw * XCH + (1852 - 1792 - 4 * r16 + 8 * g) * 2;
    unsigned aE = L + A_X + chl * XCH + (2044 - 1792) * 2 - lane * 8;
    unsigned aH5 = aH + A_H4 - lw * HCH + 128 * lw, aX5 = aXr + (4 - lw) * XCH + 128 * lw, aD = L + A_DMP + lw * 2048 + lane * 16, aF = L + L_DONE + lw * 4;
    unsigned qo = (tid >> 4) * 1280 + r16 * 16;
    PIN(xo); PIN(xa); PIN(ha); PIN(aH); PIN(aXr); PIN(aE); PIN(aH5); PIN(aX5); PIN(aD); PIN(aF); PIN(qo);
    auto hcopy = [&](int u0, int u1) {
#pragma unroll
      for (int u = u0; u < u1; u++) if (u < 2 || wv < 4) {
        const unsigned e0 = __builtin_amdgcn_alignbyte(hp[u][1], hp[u][0], 2), e1 = __builtin_amdgcn_alignbyte(hv[u][0], hp[u][1], 2), e2 = __builtin_amdgcn_alignbyte(hv[u][1], hv[u][0], 2),
                       e3 = __builtin_amdgcn_alignbyte(hv[u][2], hv[u][1], 2), e4 = __builtin_amdgcn_alignbyte(hv[u][3], hv[u][2], 2);
        const unsigned ad = u < 2 ? ha : ha + A_H4; const int o = u == 1 ? 2 * HCH : 0;
        st128(ad, o, hv[u]); st128(ad, o + HCP, (u32x4){e1, e2, e3, e4}); st128(ad, o + 2 * HCP, (u32x4){hp[u][1], hv[u][0], hv[u][1], hv[u][2]}); st128(ad, o + 3 * HCP, (u32x4){e0, e1, e2, e3}); } };
    WAITV(0);
    BAR();
    if (wv == 7 && lane == 0) set_flag(ws, FL_K + hf * 64 + ai, tag);
    ST(2);
    load_h();
    if (WOT == 0 && wv == 6) warm_wo();
    if ((tid & 31) < 24) { st128(L + (tid >> 5) * HCP + (tid & 31) * 16, 0, (u32x4){0, 0, 0, 0}); if (wv < 2) st128(L + A_H4 + (tid >> 5) * HCP + (tid & 31) * 16, 0, (u32x4){0, 0, 0, 0}); }
    else if (tid < 160) st128(L + A_X + (tid >> 5) * XCH + 4096 + (tid & 7) * 16, 0, (u32x4){0, 0, 0, 0});
    if (wv == 7) wait_flags(ws, FL_K + hf * 64 + lane, tag);
    ST(9);
    BAR();
    ST(3);
    if (wv < 5) { u32x4 xv[4]; const int xr = ROTO ? ai & 3 : 0;
#pragma unroll
      for (int u = 0; u < 4; u++) xv[u] = gld128c(rws, xo, WS_KVT + hf * 256 * KS + ai * 80 + (u ^ xr) * 64 * KS);
      SCHED();
      hcopy(0, 3);
      SCHED();
#pragma unroll
      for (int u = 0; u < 4; u++) st128(xa + (u ^ xr) * 1024, 0, xv[u]);
    } else {
      hcopy(0, 3);
      if (WOT == 1 && wv == 7) warm_wo();
    }
    auto resets = [&]() { const unsigned c1 = WS_YT + (ch0 + lw) * YS + lane * 16, c3 = WS_YT + (ch0 + 4) * YS + lw * 1024 + lane * 16, c2 = WS_O + (hf * 64 + ai) * OS + lw * 5120 + lane * 16;
#pragma unroll
        for (int i = 0; i < 5; i++) { if (CLD) __builtin_amdgcn_s_sleep(CLD); gst128c(rws, i < 4 ? c1 + i * 1024 : c3, emp); gst128c(rws, c2 + i * 1024, emp); } };
    if (CLE && wv >= 4) resets();
    const rsrc_t rq = mk(Aq, 2048 * 1280);
    u32x4 qv[3];
    unsigned qo2 = (qo + 512) | (r16 & 8) << 28; PIN(qo2);
    auto load_q = [&]() {
#pragma unroll
      for (int u = 0; u < 3; u++) qv[u] = gld128(rq, u < 2 ? qo + u * 256 : qo2, 32 * ai * 1280 + hf * 640); };
    const unsigned yo = WS_YT + (ch0 + chl) * YS;
    WAITL0(); BAR();
    ST(4);
    auto fin = [&](f32x4 a, u32x2 xr, unsigned off) {
      gst64c(rws, lane * 8 + off, yo, (u32x2){pk(a[0] + Dc * bhi(xr[1]), a[1] + Dc * blo(xr[1])), pk(a[2] + Dc * bhi(xr[0]), a[3] + Dc * blo(xr[0]))}); };
    if (wv < 4) {
      f32x4 acc[8], ac5[8];
      u32x4 F[2][8]; u32x2 G[2][8][2];
#pragma unroll
      for (int t = 0; t < 8; t++) { F[0][t] = ld128(aH5, 512 * t); G[0][t][0] = ld64(aX5, 512 * (7 - t)); SCHED(); G[0][t][1] = ld64(aX5, 512 * (7 - t) + 8); SCHED(); }
      auto step = [&](f32x4* ac, int cur, unsigned nh, unsigned nx, int off, int mode) {
        int cnt = 0;
#pragma unroll
        for (int t = 0; t < 8; t++)
#pragma unroll
          for (int p = 0; p <= t; p++) {
            const u32x4 gg = cat(G[cur][t - p][0], G[cur][t - p][1]);
            if (p == 0 && (mode == 1 || mode == 2)) MFMA_NEW(ac[t], F[cur][p], gg); else MFMA_ACC(ac[t], F[cur][p], gg);
            SCHED();
            if (cnt < 24) { const int i = cnt / 3, w = cnt % 3;
              if (w == 0) F[1 - cur][i] = ld128(nh, 512 * i + off); else G[1 - cur][i][w - 1] = ld64(nx, 512 * (7 - i) + off + 8 * (w - 1)); }
            if (mode == 2 && (cnt & 3) == 2 && cnt < 32) { const int d = cnt >> 2; st128(aD, (d >> 1) * 8192 + (d & 1) * 1024, __builtin_bit_cast(u32x4, ac5[d])); }
            if (mode == 2 && cnt == 32) *(volatile LAS unsigned*)(size_t)aF = 1u;
            if (mode == 3 && cnt == 24) { aH += 128; PIN(aH); }
            if (mode == 3 && cnt == 25) { aXr += 128; PIN(aXr); }
            if (cnt == 34) WAITL0();
            cnt++;
            SCHED();
          }
      };
      step(ac5, 0, aH5, aX5, 64, 1); step(ac5, 1, aH, aXr, 0, 0);
      step(acc, 0, aH, aXr, 64, 2); step(acc, 1, aH, aXr, 128, 3);
      load_q(); SCHED();
#pragma unroll 1
      for (int r = 0; r < 3; r++) { step(acc, 0, aH, aXr, 64, 0); step(acc, 1, aH, aXr, 128, 3); }
      u32x2 xe[8];
#pragma unroll
      for (int t = 0; t < 8; t++) { xe[t] = ld64(aE, 512 * (7 - t)); SCHED(); }
      asm volatile("" : "+v"(acc[0]), "+v"(acc[1]), "+v"(acc[2]), "+v"(acc[3]), "+v"(acc[4]), "+v"(acc[5]), "+v"(acc[6]), "+v"(acc[7]));
      ST(10);
      wait_cnt();
#pragma unroll
      for (int t = 0; t < 8; t++) { fin(acc[t], xe[t], 512 * t); SCHED(); }
    } else {
      __builtin_amdgcn_s_setprio(1);
      if (!CLE) resets();
      {
        WAITV(0); arrive(); }
      wait_cnt();
      if (wv == 7 && lane == 0) set_flag(ws, FL_A + hf * 64 + ai, tag);
#pragma unroll 1
      for (int it = 0; it < (1 << 20) && !__all(*(volatile LAS unsigned*)(size_t)(L + L_DONE + p4 * 4) == 1u); it++) __builtin_amdgcn_s_sleep(2);
      asm volatile("" ::: "memory");
      { const unsigned rb_ = L + A_DMP + lw * 8192 + lane * 16;
#pragma unroll
        for (int e = 0; e < 2; e++) { f32x4 sm = __builtin_bit_cast(f32x4, ld128(rb_, e * 1024));
#pragma unroll
          for (int w = 1; w < 4; w++) { const f32x4 o = __builtin_bit_cast(f32x4, ld128(rb_, w * 2048 + e * 1024));
#pragma unroll
            for (int i = 0; i < 4; i++) sm[i] = add1(sm[i], o[i]); }
          const u32x2 xr = ld64(aE + 512 * (7 - 2 * lw - e), 0);
          gst64c(rws, lane * 8 + 512 * (2 * lw + e), yo, (u32x2){pk(fma1(Dc, bhi(xr[1]), sm[0]), fma1(Dc, blo(xr[1]), sm[1])), pk(fma1(Dc, bhi(xr[0]), sm[2]), fma1(Dc, blo(xr[0]), sm[3]))}); } }
      load_q();
      if (wv == 7) {
#pragma unroll 1
        for (int it = 0; it < (1 << 18); it++) {
          bool ok = ld_flag(ws, FL_A + hf * 64 + lane) == tag;
          if (b == 128) ok = ok & (ld_flag(ws, lane) == tag) & (ld_flag(ws, 64 + lane) == tag) & (ld_flag(ws, 128 + lane) == tag) & (ld_flag(ws, 192 + lane) == tag);
          if (__all(ok)) break;
          __builtin_amdgcn_s_sleep(4);
        }
        asm volatile("" ::: "memory");
        if (b == 128 && lane == 0) __hip_atomic_store((GAS unsigned*)(ws + WS_EPOCH), tag, __ATOMIC_RELAXED, __HIP_MEMORY_SCOPE_AGENT);
      }
      __builtin_amdgcn_s_setprio(0);
    }
    ST(5);
    WAITV(0); WAITL0(); BAR();
    ST(11);
    { u32x4 yv[3];
      const unsigned yl = (tid >> 2) * YS + p4 * 16, ys = L + T_SCR + p4 * (8 * 640 + 32) + (tid >> 2) * 2;
#pragma unroll 1
      for (int it = 0; it < SWEEPS; it++) {
        unsigned m = 0;
#pragma unroll
        for (int u = 0; u < 3; u++) if (u < 2 || wv < 4) yv[u] = gld128c(rws, yl, WS_YT + (320 * hf + 128 * u) * YS + ai * 64);
#pragma unroll
        for (int u = 0; u < 3; u++) if (u < 2 || wv < 4) m = mx4(m, yv[u]);
        asm volatile("" ::: "memory");
        if (none_empty(m)) break;
      }
      ST(6);
#pragma unroll
      for (int u = 0; u < 3; u++) if (u < 2 || wv < 4) {
#pragma unroll
        for (int e = 0; e < 4; e++) { st16(ys, 256 * u + 1280 * e, (u16)yv[u][e]); st16(ys, 256 * u + 1280 * e + 640, (u16)(yv[u][e] >> 16)); } } }
    WAITL0(); BAR();
    ST(12);
    { const unsigned ya = L + T_SCR + (tid >> 4) * 640 + (wv >> 1) * 32 + r16 * 16, oo = (tid >> 4) * 2560 + r16 * 16 + 32 * ai * 2560 + hf * 640;
      u32x4 y[3];
#pragma unroll
      for (int u = 0; u < 3; u++) y[u] = ld128(ya, 256 * u);
      SCHED();
#pragma unroll
      for (int u = 0; u < 3; u++) { u32x4 r;
#pragma unroll
        for (int e = 0; e < 4; e++) r[e] = pk(blo(y[u][e]) * blo(qv[u][e]), bhi(y[u][e]) * bhi(qv[u][e]));
        __builtin_amdgcn_raw_buffer_store_b128(r, ro, u < 2 ? oo + u * 256 : (oo + 512) | (r16 & 8) << 28, 0, OUTAUX); } }
  }
  ST(7); STEND();
}

extern "C" void kernel_launch(void* const* d_in, const int* in_sizes, int n_in, void* d_out, int out_size, void* d_ws, size_t ws_size, hipStream_t stream) {
  fused_kernel<<<256, 512, 0, stream>>>((const u16*)d_in[0], (const u16*)d_in[1], (const u16*)d_in[2], (const u16*)d_in[3], (const u16*)d_in[4], (const u16*)d_in[5],
                                        (const u16*)d_in[6], (const u16*)d_in[7], (const u16*)d_in[8], (u16*)d_out, (char*)d_ws);
}
```

```cpp
#include <hip/hip_runtime.h>
#include <hip/hip_bf16.h>

typedef unsigned short u16;
typedef unsigned long long u64;
typedef short s16x4 __attribute__((ext_vector_type(4)));
typedef short s16x8 __attribute__((ext_vector_type(8)));
typedef __bf16 bf16x2 __attribute__((ext_vector_type(2)));
typedef __bf16 bf16x8 __attribute__((ext_vector_type(8)));
typedef float f32x2 __attribute__((ext_vector_type(2)));
typedef float f32x4 __attribute__((ext_vector_type(4)));
typedef unsigned u32x2 __attribute__((ext_vector_type(2)));
typedef unsigned u32x4 __attribute__((ext_vector_type(4)));
typedef int i32x4 __attribute__((ext_vector_type(4)));
#define LAS __attribute__((address_space(3)))
#define GAS __attribute__((address_space(1)))
#define DI __device__ __forceinline__
#define WAITV(n) asm volatile("s_waitcnt vmcnt(" #n ")" ::: "memory")
#define WAITL0() __builtin_amdgcn_s_waitcnt(0xC07F)
#define WAITL(n) __builtin_amdgcn_s_waitcnt(0xC07F | ((n) << 8))
#define BAR() do { asm volatile("" ::: "memory"); __builtin_amdgcn_s_barrier(); asm volatile("" ::: "memory"); } while (0)
#define SCHED() __builtin_amdgcn_sched_barrier(0)
#define P1 6
#define WR 15
#define CLD 0
#define WOT 0
#define ROTO 1
#define P0 0
#define HB 0
#define WQB 1
#define YD 0
#define GR 0
#define WO0 0
#define EARLY 1
#define RETRY 100
#define PG 0
#define OCL 2
#define GD 0
#define B11 1
#define HPRIO 1
#define CLE 0
#define SPEC 2
#define SD 0
#define CPRIO 1
#define PRE 1
#define LEAP 1
#define OUTA 16
#define OUTB 0
#define CODEB 34304
#define STAMP 0

DI f32x4 mfma16(u32x4 a, u32x4 b, f32x4 c) { return __builtin_amdgcn_mfma_f32_16x16x32_bf16(__builtin_bit_cast(bf16x8, a), __builtin_bit_cast(bf16x8, b), c, 0, 0, 0); }
DI u32x4 cat(u32x2 a, u32x2 b) { return __builtin_shufflevector(a, b, 0, 1, 2, 3); }
DI u32x4 ld128(unsigned a, int off) { return *(const LAS u32x4*)((LAS char*)(size_t)a + off); }
DI u32x2 ld64(unsigned a, int off) { return *(const LAS u32x2*)((LAS char*)(size_t)a + off); }
DI u32x2 ldtr(unsigned a, int off) { return __builtin_bit_cast(u32x2, __builtin_amdgcn_ds_read_tr16_b64_v4i16((LAS s16x4*)((LAS char*)(size_t)a + off))); }
DI void st128(unsigned a, int off, u32x4 v) { *(LAS u32x4*)((LAS char*)(size_t)a + off) = v; }
DI void st64(unsigned a, int off, u32x2 v) { *(LAS u32x2*)((LAS char*)(size_t)a + off) = v; }
DI void st16(unsigned a, int off, u16 v) { *(LAS u16*)((LAS char*)(size_t)a + off) = v; }
DI unsigned pk(float lo, float hi) { f32x2 v = {lo, hi}; return __builtin_bit_cast(unsigned, __builtin_convertvector(v, bf16x2)); }
DI float blo(unsigned u) { return __uint_as_float(u << 16); }
DI float bhi(unsigned u) { return __uint_as_float(u & 0xffff0000u); }
DI unsigned phi2(float u0, float u1) {
  const f32x2 u = {u0, u1}, a = u * 1.44269504088896341f - 1.44269504088896341f;
  f32x2 p = {__builtin_amdgcn_fmed3f(u0, __builtin_amdgcn_exp2f(a[0]), 1.f), __builtin_amdgcn_fmed3f(u1, __builtin_amdgcn_exp2f(a[1]), 1.f)};
  p += 1e-6f;
  return pk(p[0], p[1]);
}
DI float add1(float a, float b) { float r = a + b; asm("" : "+v"(r)); return r; }
DI float mul1(float a, float b) { float r = a * b; asm("" : "+v"(r)); return r; }
DI float fma1(float a, float b, float c) { float r = __builtin_fmaf(a, b, c); asm("" : "+v"(r)); return r; }
#define MFMA_NEW(acc, a, b) asm("v_mfma_f32_16x16x32_bf16 %0, %1, %2, 0" : "=&v"(acc) : "v"(a), "v"(b))
#define MFMA_ONE(acc, a, b) asm("v_mfma_f32_16x16x32_bf16 %0, %1, %2, 1.0" : "=&v"(acc) : "v"(a), "v"(b))
#define MFMA_INI(acc, a, b, c) asm("v_mfma_f32_16x16x32_bf16 %0, %1, %2, %3" : "=&v"(acc) : "v"(a), "v"(b), "v"(c))
#define MFMA_ACC(acc, a, b) asm("v_mfma_f32_16x16x32_bf16 %0, %1, %2, %0" : "+v"(acc) : "v"(a), "v"(b))
DI i32x4 make_srd(const void* p, unsigned bytes) { u64 a = (u64)p; i32x4 r; r[0] = (int)(unsigned)a; r[1] = (int)(unsigned)(a >> 32) & 0xffff; r[2] = (int)bytes; r[3] = 0x00020000; return r; }
DI void dma16(i32x4 srd, unsigned voff, unsigned soff, unsigned m0v) {
  asm volatile("s_mov_b32 m0, %3\n\ts_nop 0\n\tbuffer_load_dwordx4 %0, %1, %2 offen lds" ::"v"(voff), "s"(srd), "s"(soff), "s"(m0v) : "memory");
}
DI void dma16x3(i32x4 srd, unsigned v0, unsigned v1, unsigned v2, unsigned soff, unsigned m0v) {
  asm volatile("s_mov_b32 m0, %5\n\ts_nop 0\n\tbuffer_load_dwordx4 %0, %3, %4 offen lds\n\tbuffer_load_dwordx4 %1, %3, %4 offen offset:1024 lds\n\tbuffer_load_dwordx4 %2, %3, %4 offen offset:2048 lds"
               ::"v"(v0), "v"(v1), "v"(v2), "s"(srd), "s"(soff), "s"(m0v) : "memory");
}
DI void touch(i32x4 srd, unsigned voff, unsigned soff, unsigned m0v) {
  asm volatile("s_mov_b32 m0, %3\n\ts_nop 0\n\tbuffer_load_dword %0, %1, %2 offen lds" ::"v"(voff), "s"(srd), "s"(soff), "s"(m0v) : "memory");
}
typedef __amdgpu_buffer_rsrc_t rsrc_t;
DI rsrc_t mk(const void* p, unsigned bytes) { return __builtin_amdgcn_make_buffer_rsrc((void*)p, 0, bytes, 0x00020000); }
constexpr int SC1 = 16;
DI u32x4 gld128(rsrc_t r, unsigned v, unsigned s) { return __builtin_amdgcn_raw_buffer_load_b128(r, v, s, 0); }
DI u32x4 gld128c(rsrc_t r, unsigned v, unsigned s) { return __builtin_amdgcn_raw_buffer_load_b128(r, v, s, SC1); }
DI u32x2 gld64c(rsrc_t r, unsigned v, unsigned s) { return __builtin_amdgcn_raw_buffer_load_b64(r, v, s, SC1); }
DI void gst128c(rsrc_t r, unsigned v, u32x4 d) { __builtin_amdgcn_raw_buffer_store_b128(d, r, v, 0, SC1); }
DI void gst64c(rsrc_t r, unsigned v, unsigned s, u32x2 d) { __builtin_amdgcn_raw_buffer_store_b64(d, r, v, s, SC1); }

constexpr unsigned WS_EPOCH = 0;
constexpr unsigned WS_FLAG = 4096;
constexpr unsigned FL_G = 0;
constexpr unsigned FL_K = 128;
constexpr unsigned FL_A = 256;
constexpr unsigned FL_O = 384;
#define PRS 4480
#define YS 4480
#define KS 5248
#define OS 20864
constexpr unsigned WS_PART = 1u << 18;
constexpr unsigned WS_O = 1u << 20;
constexpr unsigned WS_KVT = 4u << 20;
constexpr unsigned WS_YT = 8u << 20;
constexpr unsigned WS_DBG = 12u << 20;
constexpr unsigned WS_BYTES = 16u << 20;

DI void set_flag(char* ws, unsigned idx, unsigned tag) { __hip_atomic_store((GAS unsigned*)(ws + WS_FLAG + idx * 128), tag, __ATOMIC_RELAXED, __HIP_MEMORY_SCOPE_AGENT); }
DI void wait_flags(char* ws, unsigned idx, unsigned tag) {
  GAS unsigned* p = (GAS unsigned*)(ws + WS_FLAG + idx * 128);
#pragma unroll 1
  for (int spins = 0; spins < (1 << 18); ++spins) {
    const unsigned v = __hip_atomic_load(p, __ATOMIC_RELAXED, __HIP_MEMORY_SCOPE_AGENT);
    if (__all(v == tag)) break;
    __builtin_amdgcn_s_sleep(1);
  }
  asm volatile("" ::: "memory");
}
DI unsigned ld_flag(char* ws, unsigned idx) { return __hip_atomic_load((GAS unsigned*)(ws + WS_FLAG + idx * 128), __ATOMIC_RELAXED, __HIP_MEMORY_SCOPE_AGENT); }
constexpr unsigned EMPTY = 0xffffffffu;
constexpr int SWEEPS = 1 << 16;
DI unsigned umax3(unsigned a, unsigned b, unsigned c) { return __builtin_elementwise_max(__builtin_elementwise_max(a, b), c); }
DI unsigned mx4(unsigned m, u32x4 v) { return umax3(umax3(m, v[0], v[1]), v[2], v[3]); }
DI bool none_empty(unsigned m) { return __builtin_amdgcn_ballot_w64(m == EMPTY) == 0; }
#define PIN(x) asm volatile("" : "+v"(x))
#define EV(k)
#define ST(n) SCHED()
#define STEND()

constexpr int SLOT = 40960, XB = 16384, WB = 12288;
constexpr int G_RED = 36864;
constexpr int L_DONE = 161792;
constexpr int L_CNT = 161808;
constexpr int L_PAIR = 161824;
constexpr int L_DUMMY = 163328;
constexpr int WOP[4] = {131072, 45056, 81920, 0};
constexpr int G_OUT = 112640;
constexpr int HCP = 4640, HCH = 4 * HCP;
constexpr int A_X = 4 * HCH, XCH = 4224;
constexpr int A_H4 = A_X + 5 * XCH;
constexpr int KP = 36 * 256, VP = 32 * 256;
constexpr int A_K = A_X, A_V = A_X + 3 * KP;
constexpr int A_DMP = 113920;
constexpr int T_SCR = 61440;

__global__ void __launch_bounds__(512, 2)
fused_kernel(const u16* __restrict__ Aq, const u16* __restrict__ Ak, const u16* __restrict__ Av, const u16* __restrict__ Ask,
             const u16* __restrict__ Ah, const u16* __restrict__ AD, const u16* __restrict__ Bx, const u16* __restrict__ Bw,
             const u16* __restrict__ Bo, u16* __restrict__ out, char* __restrict__ ws) {
  __shared__ __attribute__((aligned(16))) char smem[163840];
  const int b = blockIdx.x, xcd = b & 7, j = (b >> 3) & 15;
  int role = b >> 7;
  const int tid = threadIdx.x, lane = tid & 63;
  int wv = __builtin_amdgcn_readfirstlane(tid >> 6);
  const unsigned L = (unsigned)(size_t)(LAS char*)smem;
  unsigned stampv = 0; (void)stampv;
  ST(0);
  asm volatile("" ::"s"(Aq), "s"(Ak), "s"(Av), "s"(Ask), "s"(Ah), "s"(AD), "s"(Bx), "s"(Bw), "s"(Bo), "s"(out), "s"(ws));
  const int rb = (xcd >> 1) * 4 + (j >> 2), cg = (xcd & 1) * 4 + (j & 3);
  const int a0 = (160 * cg) & ~63, dl = 160 * cg - a0;
  const int hf = xcd & 1, ai = (xcd >> 1) * 16 + j;
  const i32x4 sx = make_srd(Bx, 2048 * 640 * 2), sw = make_srd(Bw, 640 * 1920 * 2);

  unsigned va[3], vb[3], vx = 0;
  int sp = 0;
  u32x2 wk;
  auto issue_x = [&](int slot) {
#pragma unroll
    for (int u = 0; u < 4; u++) dma16(sx, vx, sp * 128 + u * 32 * 1280, L + slot * SLOT + ((wv & 3) + 4 * u) * 1024);
  };
  auto issue_w = [&](int slot) {
    const int lw = wv & 3; const unsigned lb = L + slot * SLOT, so = (sp * 64 + 16 * lw) * 3840 + 1280 + a0;
    dma16x3(sw, va[0], va[1], va[2], so, lb + XB + lw * 3072);
    dma16x3(sw, vb[0], vb[1], vb[2], so + 1280, lb + XB + WB + lw * 3072);
    sp++;
  };
  auto set_warm = [&]() { const unsigned r = (lane * 26) >> 8, l = lane - r * 10;
    va[0] = (((xcd >> 1) * 512 + 32 * j + lane) * 1280) | (lane < 32 ? 0u : 0xf0000000u);
    va[1] = ((4 * j + r) * 3840 + (l >= 5 ? 640 : 0) + l * 128) | (lane < 40 ? 0u : 0xf0000000u); };
  auto warm = [&](int s) { touch(sx, va[0], s * 128, L + L_DUMMY); touch(sw, va[1], s * 64 * 3840 + 1280 + (xcd & 1) * 640, L + L_DUMMY); };
  const rsrc_t rws = mk(ws, WS_BYTES);
  unsigned tag = 0;
  if (wv == 7) {
    tag = *(const __attribute__((address_space(4))) unsigned*)(ws + WS_EPOCH) + 1u;
    if (role == 0) {
#pragma unroll
      for (int i = 0; i < 4; i++) gst128c(rws, WS_PART + (cg * 16 + rb) * PRS + lane * 16 + i * 1024, (u32x4){EMPTY, EMPTY, EMPTY, EMPTY});
    }
    asm volatile("" : "+s"(tag));
  }
  if (role == 0) {
    if (wv < 4) {
      { const int row = 8 * wv + (lane >> 3); vx = (rb * 128 + row) * 1280 + (((lane & 7) ^ ((row >> 1) & 7)) << 4); }
      ST(13);
      issue_x(0);
#pragma unroll
      for (int u = 0; u < 3; u++) { const unsigned c = u * 64 + lane, row = (c * 171) >> 11, pos = c - row * 12; va[u] = row * 3840 + ((pos ^ ((row >> 3) << 1)) << 4) - 1024 * u; vb[u] = va[u] ^ 16; }
      issue_w(0);
    } else if (wv == 6) {
      u64 pc; asm volatile("s_getpc_b64 %0" : "=s"(pc));
      touch(make_srd((const void*)(pc & ~127ull), CODEB), (lane * 16 + j) * 128, 0, L + L_DUMMY);
    }
  } else {
    const i32x4 sk_ = make_srd(Ak, 2048 * 1280), sv_ = make_srd(Av, 2048 * 1280);
    const unsigned rl = lane >> 4, base = (32 * ai + 4 * wv + rl) * 1280 + hf * 640;
    const unsigned ck = ((lane ^ rl ^ (((wv + 1) & 6) << 1)) & 15) << 4, cv = ((lane ^ rl ^ ((wv & 6) << 1)) & 15) << 4;
    if (wv + P0 <= P1) { set_warm(); warm(wv + P0); }
    asm volatile("global_load_dwordx2 %0, %1, %2" : "=v"(wk) : "v"((320 * hf + 16 * wv + (rl < 2 || (rl == 2 && wv < 4) ? 128 * rl : 0) + (lane & 15)) * 8), "s"(Ask) : "memory");
#pragma unroll
    for (int u = 0; u < 3; u++) {
      dma16(sk_, (base + ck) | (u == 2 ? (ck & 128) << 24 : 0), u * 256, L + A_K + u * KP + (wv + 1) * 1024);
      dma16(sv_, (base + cv) | (u == 2 ? (cv & 128) << 24 : 0), u * 256, L + A_V + u * VP + wv * 1024); }
    if (wv < 3) { const unsigned c0 = ((lane ^ rl) & 15) << 4;
      dma16(sk_, ((32 * ai - 4 + rl) * 1280 + hf * 640 + c0 + wv * 256) | (wv == 2 ? (c0 & 128) << 24 : 0), 0, L + A_K + wv * KP); }
    ST(13);
  }
  asm volatile("" : "+s"(role), "+s"(wv));
  const int lw = wv & 3, g = lane >> 4, r16 = lane & 15, q4 = r16 >> 2, p4 = lane & 3;
  const rsrc_t ro = mk(out, 2048 * 1280 * 2);
  auto wait_cnt = [&](unsigned need = 4) {
#pragma unroll 1
    for (int it = 0; it < (1 << 20) && __builtin_amdgcn_readfirstlane(*(volatile LAS unsigned*)(size_t)(L + L_CNT)) < need; it++) __builtin_amdgcn_s_sleep(1);
    asm volatile("" ::: "memory"); };
  auto arrive = [&]() { if (lane == 0) __hip_atomic_fetch_add((LAS unsigned*)(size_t)(L + L_CNT), 1u, __ATOMIC_RELAXED, __HIP_MEMORY_SCOPE_WORKGROUP); };
  const u32x4 emp = {EMPTY, EMPTY, EMPTY, EMPTY};

  if (role == 0) {
    const int cg3 = cg, rt0 = rb * 8, a3 = a0, dl3 = dl;
    if (wv < 4) {
      auto issue_q = [&](int pos) {
        const unsigned so = ((sp - 10) * 64 + 16 * lw) * 3840 + a0;
        dma16x3(sw, va[0], va[1], va[2], so, L + pos + lw * 3072);
        sp++;
      };
      sp = 1;
      PIN(vb[0]); PIN(vb[1]); PIN(vb[2]);
      issue_x(1); issue_w(1);
      ST(15);
      unsigned aX[2]; { const int row = lw * 32 + r16;
#pragma unroll
        for (int kk = 0; kk < 2; kk++) aX[kk] = L + row * 128 + (((4 * kk + g) ^ ((row >> 1) & 7)) << 4); }
      u32x4 xq[10][2][2];
      constexpr int QP[10] = {45056, 45056 + WB, 45056 + 2 * WB, 81920, 81920 + WB, 81920 + 2 * WB, 81920 + 3 * WB, 0, WB, 2 * WB};
      unsigned aQ[5];
#pragma unroll
      for (int n = 0; n < 5; n++) { const int off = (n < 4 ? dl + 40 * n + 8 * p4 : dl + 40 * p4 + 32) ^ ((g & 1) << 5); aQ[n] = L + QP[0] + (8 * g + q4) * 192 + off; PIN(aQ[n]); }
      unsigned aE[6]; { const int col = p4 == 0 ? 8 + q4 : 0; aE[0] = L + G_RED + lane * 16; aE[1] = L + G_RED + r16 * 16; aE[2] = L + G_RED + (16 * g + col) * 16; aE[3] = L + G_RED + col * 16;
        aE[4] = aE[2] ^ 32; aE[5] = aE[3] ^ 32; }
      const unsigned ob = WS_O + (rb * 8 + lw * 2) * OS + (cg >> 1) * 5120;
      unsigned o16[2] = {ob + (cg & 1) * 2048 + lane * 16, ob + (cg & 1) * 2048 + lane * 16 + OS}, o8 = ob + 4096 + (cg & 1) * OS + lane * 16;
      PIN(aE[0]); PIN(aE[1]); PIN(aE[2]); PIN(aE[3]); PIN(aE[4]); PIN(aE[5]); PIN(o16[0]); PIN(o16[1]); PIN(o8);
      unsigned aP = L + (lane ? L_DUMMY + lane * 4 : L_DONE); PIN(aP);
#pragma unroll
      for (int s = 0; s < 10; s++) {
        if (s < 9) WAITV(10); else WAITV(9);
        WAITL0(); BAR();
        if (s < 8) issue_x((s + 2) % 3); else issue_q(QP[s == 8 ? 0 : 3]);
#pragma unroll
        for (int kk = 0; kk < 2; kk++)
#pragma unroll
          for (int m = 0; m < 2; m++) xq[s][kk][m] = ld128(aX[kk] + (s % 3) * SLOT, m * 2048);
        if (s < 8) issue_w((s + 2) % 3);
        else if (s == 8) { issue_q(QP[1]); issue_q(QP[2]); }
        else { issue_q(QP[4]); issue_q(QP[5]); }
      }
      WAITL0(); BAR();
      issue_q(QP[6]); issue_q(QP[7]); issue_q(QP[8]); issue_q(QP[9]);
      f32x4 qa[2][5];
      ST(1);
      {
        u32x2 fw[2][5][2];
        auto rdw = [&](int ks, int n, int h) {
          if (h == 0 && ks == 6) { aQ[n] += QP[3] - QP[0]; asm volatile("" : "+v"(aQ[n])); }
          if (h == 0 && ks == 14) { aQ[n] -= QP[3]; asm volatile("" : "+v"(aQ[n])); }
          fw[ks & 1][n][h] = ldtr(aQ[n], (ks - (ks < 6 ? 0 : ks < 14 ? 6 : 14)) * 32 * 192 + 768 * h); };
        ST(8); WAITV(0); BAR(); ST(10);
#pragma unroll
        for (int n = 0; n < 5; n++) { rdw(0, n, 0); rdw(0, n, 1); }
        SCHED();
#pragma unroll
        for (int ks = 0; ks < 20; ks++) {
          if (ks == 5 || ks == 13 || ks == 19) { asm volatile("ds_add_u32 %0, %1" ::"v"(aP), "v"(1u << (ks == 5 ? 0 : ks == 13 ? 8 : 16)) : "memory"); SCHED(); }
#pragma unroll
          for (int n = 0; n < 5; n++) {
            if (n == 0) WAITL(4); else if (n == 3) WAITL(6);
            SCHED();
            const u32x4 w = cat(fw[ks & 1][n][0], fw[ks & 1][n][1]);
            if (ks) MFMA_ACC(qa[0][n], w, xq[ks >> 1][ks & 1][0]); else MFMA_ONE(qa[0][n], w, xq[0][0][0]);
            SCHED();
            if (ks < 19) rdw(ks + 1, n, 0);
            SCHED();
            if (ks) MFMA_ACC(qa[1][n], w, xq[ks >> 1][ks & 1][1]); else MFMA_ONE(qa[1][n], w, xq[0][0][1]);
            SCHED();
            if (ks < 19) rdw(ks + 1, n, 1);
            SCHED();
          }
        }
        asm volatile("s_nop 7" : "+v"(qa[0][4]), "+v"(qa[1][4]), "+v"(qa[0][3]), "+v"(qa[1][3]), "+v"(qa[0][2]), "+v"(qa[1][2]), "+v"(qa[0][1]), "+v"(qa[1][1]), "+v"(qa[0][0]), "+v"(qa[1][0]));
      }
      ST(11);
      u32x4 bq[2][4];
      u32x2 e0[4][2], e1[4][2];
      f32x4 n0[2][4], n1[2][4]; float rc[2][4];
      auto omul = [&](int m, int i) { const int h = i & 3;
        if (i < 4) n1[m][h] = mfma16(cat(e1[h][0], e1[h][1]), bq[m][h], (f32x4){0, 0, 0, 0}); else n0[m][h] = mfma16(cat(e0[h][0], e0[h][1]), bq[m][h], (f32x4){0, 0, 0, 0});
        SCHED(); };
      auto get_state = [&]() {
        asm volatile("ds_add_u32 %0, %1" ::"v"(aP), "v"(1u << 24) : "memory");
        ST(2); BAR(); ST(3);
#pragma unroll
        for (int h = 0; h < 4; h++) { e1[h][0] = ld64(aE[h < 2 ? 2 : 4], (h >> 1) * 2048 + (h & 1) * 64); SCHED(); e1[h][1] = ld64(aE[h < 2 ? 3 : 5], (h >> 1) * 2048 + (h & 1) * 320 + 520); SCHED(); }
#pragma unroll
        for (int h = 0; h < 4; h++) { e0[h][0] = ld64(aE[0], 4096 + (h >> 1) * 2048 + 8 * (h & 1)); SCHED(); e0[h][1] = ld64(aE[1], (h >> 1) * 2048 + (h & 1) * 256 + 8); SCHED(); } };
#pragma unroll
      for (int m = 0; m < 2; m++) {
        f32x2 u[10], a[10];
#pragma unroll
        for (int i = 0; i < 10; i++) { u[i] = (f32x2){qa[m][i >> 1][2 * (i & 1)], qa[m][i >> 1][2 * (i & 1) + 1]}; a[i] = u[i] * 1.44269504088896341f - 1.44269504088896341f; }
        SCHED();
#pragma unroll
        for (int i = 0; i < 10; i++) { a[i] = (f32x2){__builtin_amdgcn_exp2f(a[i][0]), __builtin_amdgcn_exp2f(a[i][1])}; SCHED(); if (EARLY && m && i >= 2) omul(0, i - 2); }
#pragma unroll
        for (int i = 0; i < 10; i++) a[i] = (f32x2){__builtin_amdgcn_fmed3f(u[i][0], a[i][0], 1.f), __builtin_amdgcn_fmed3f(u[i][1], a[i][1], 1.f)};
        SCHED();
#pragma unroll
        for (int i = 0; i < 10; i++) a[i] += 1e-6f;
        const unsigned t0 = pk(a[8][0], a[8][1]), t1 = pk(a[9][0], a[9][1]);
#pragma unroll
        for (int h = 0; h < 4; h++) { bq[m][h] = (u32x4){pk(a[2 * h][0], a[2 * h][1]), pk(a[2 * h + 1][0], a[2 * h + 1][1]), g == h ? t0 : 0u, g == h ? t1 : 0u}; PIN(bq[m][h]); }
        SCHED();
        if (m == (EARLY ? 0 : 1)) get_state();
      }
      if (!EARLY)
#pragma unroll
        for (int i = 0; i < 8; i++) omul(0, i);
      const unsigned pw = *(volatile LAS unsigned*)(size_t)aP;
      SCHED();
#pragma unroll
      for (int i = 0; i < 8; i++) { omul(1, i); if (i & 1) rc[0][i >> 1] = __builtin_amdgcn_rcpf(rc[0][i >> 1]); else rc[0][i >> 1] = add1(n1[0][i >> 1][1], 1e-6f); SCHED(); }
      if (!(__builtin_amdgcn_readfirstlane(pw) & 0x8000)) {
#pragma unroll 1
        for (int it = 0; it < (1 << 22) && !(__builtin_amdgcn_readfirstlane(*(volatile LAS unsigned*)(size_t)aP) & 0x8000); it++) __builtin_amdgcn_s_sleep(1);
        asm volatile("" ::: "memory"); }
#pragma unroll
      for (int m = 0; m < 2; m++) {
        if (m)
#pragma unroll
          for (int h = 0; h < 4; h++) rc[1][h] = __builtin_amdgcn_rcpf(n1[1][h][1] + 1e-6f);
#pragma unroll
        for (int hp_ = 0; hp_ < 2; hp_++) { const f32x4 a = n0[m][2 * hp_] * rc[m][2 * hp_], c2 = n0[m][2 * hp_ + 1] * rc[m][2 * hp_ + 1];
          gst128c(rws, o16[m] + hp_ * 1024, (u32x4){pk(a[0], a[1]), pk(a[2], a[3]), pk(c2[0], c2[1]), pk(c2[2], c2[3])}); SCHED(); }
      }
      gst128c(rws, o8, (u32x4){pk(mul1(n1[0][0][0], rc[0][0]), mul1(n1[0][1][0], rc[0][1])), pk(mul1(n1[0][2][0], rc[0][2]), mul1(n1[0][3][0], rc[0][3])),
                               pk(mul1(n1[1][0][0], rc[1][0]), mul1(n1[1][1][0], rc[1][1])), pk(mul1(n1[1][2][0], rc[1][2]), mul1(n1[1][3][0], rc[1][3]))});
      asm volatile("ds_add_u32 %0, %1 offset:16" ::"v"(aP), "v"(1u) : "memory");
      ST(12);
    } else {
      const int rg = lw >> 1, c = lw & 1;
      unsigned aA[2]; { const int row = rg * 64 + r16;
#pragma unroll
        for (int kk = 0; kk < 2; kk++) aA[kk] = L + row * 128 + (((4 * kk + g) ^ ((row >> 1) & 7)) << 4); }
      unsigned aB[5];
#pragma unroll
      for (int n = 0; n < 5; n++) { int sec, off; if (n < 4) { sec = n >> 1; off = dl + 80 * c + 40 * (n & 1) + 8 * p4; } else { sec = p4 >> 1; off = dl + 80 * c + 40 * (p4 & 1) + 32; }
        off ^= ((g & 1) << 5) ^ (sec << 4); aB[n] = L + XB + sec * WB + (8 * g + q4) * 192 + off; }
      if (lane < 12) *(LAS unsigned*)(size_t)(L + L_DONE + lane * 4) = 0;
      f32x4 acc[4][5];
      u32x4 fa[2][4]; u32x2 fb[2][5][2];
      const float i4 = r16 < 8 ? 1.f : 0.f;
      f32x4 c4 = {i4, i4, i4, i4}; PIN(c4);
      unsigned aA2[2], aB2[5];
#pragma unroll
      for (int i = 0; i < 2; i++) { aA2[i] = aA[i] + 2 * SLOT; PIN(aA[i]); PIN(aA2[i]); }
#pragma unroll
      for (int i = 0; i < 5; i++) { aB2[i] = aB[i] + 2 * SLOT; PIN(aB[i]); PIN(aB2[i]); }
      auto rd = [&](int t, int slot, int kk) {
        const int o = slot == 1 ? SLOT : 0;
        if (t < 4) fa[kk][t] = ld128(slot == 2 ? aA2[kk] : aA[kk], o + t * 2048);
        else { const int n2 = (t - 4) >> 1, h = (t - 4) & 1; fb[kk][n2][h] = ldtr(slot == 2 ? aB2[n2] : aB[n2], o + kk * 32 * 192 + h * 768); } };
      auto half = [&](int slot, int kk, bool first = false) {
#pragma unroll
        for (int t = 0; t < 20; t++) {
          const u32x4 w = cat(fb[1 - kk][t >> 2][0], fb[1 - kk][t >> 2][1]);
          if (!first) MFMA_ACC(acc[t & 3][t >> 2], fa[1 - kk][t & 3], w);
          else if (t < 8) MFMA_ONE(acc[t & 3][t >> 2], fa[1 - kk][t & 3], w);
          else if (t < 16) MFMA_NEW(acc[t & 3][t >> 2], fa[1 - kk][t & 3], w);
          else MFMA_INI(acc[t & 3][t >> 2], fa[1 - kk][t & 3], w, c4);
          SCHED();
          if (t < 14) rd(t, slot, kk);
          if (t == 18) WAITL0();
          SCHED();
        }
      };
      unsigned vo[3], aW[3][3][2], go[5], pvo = g * PRS + r16 * 16, aS = L + G_RED + (lw ^ 2) * 2048 + ((g >> 1) * 16 + r16) * 16 + (g & 1) * 8;
      u32x4 sel[2];
#pragma unroll
      for (int u = 0; u < 3; u++) { const unsigned cc = u * 64 + lane, row = (cc * 171) >> 11, ps = cc - row * 12; vo[u] = row * 1280 + ((ps ^ (((row >> 2) & 1) << 1)) << 4) - 1024 * u; PIN(vo[u]); }
#pragma unroll
      for (int o = 0; o < 2; o++) {
#pragma unroll
        for (int i = 0; i < 4; i++) sel[o][i] = ((r16 >> 1) == 4 * o + i) ? 0x3f80u << ((r16 & 1) * 16) : 0u;
        PIN(sel[o]); }
#pragma unroll
      for (int e = 0; e < 2; e++)
#pragma unroll
        for (int r = 0; r < 3; r++) {
          aW[2][r][e] = L + WOP[3] + (r < 2 ? 4 * g + q4 : 20 * q4 + 16 + (g ^ (q4 & 2))) * 192 + ((dl3 + 32 * e + 8 * p4) ^ (((r < 2 ? g + r : q4) & 1) << 5));
          aW[0][r][e] = aW[2][r][e] + WOP[0] - WOP[3]; aW[1][r][e] = aW[2][r][e] + WOP[1] - WOP[3]; PIN(aW[0][r][e]); PIN(aW[1][r][e]); PIN(aW[2][r][e]); }
#pragma unroll
      for (int u = 0; u < 5; u++) { const unsigned c = lane + 64 * u, row = (c * 6554) >> 16; go[u] = row * 2400 + c * 16 + (rt0 + 2 * lw) * 16 * 2560 + 1280 + cg3 * 160; PIN(go[u]); }
      PIN(pvo); PIN(aS);
      const int h1 = xcd & 1;
      unsigned fv = 0;
      __builtin_amdgcn_s_setprio(2);
      WAITL0(); BAR(); ST(14);
#pragma unroll
      for (int t = 0; t < 14; t++) { rd(t, 0, 0); SCHED(); }
      WAITL0(); SCHED();
      half(0, 1, true);
#pragma unroll 1
      for (int s = 0; s < 3; s++) {
        BAR(); SCHED(); half(1, 0); half(1, 1);
        BAR(); SCHED(); half(2, 0); half(2, 1);
        BAR(); if (s == 0) ST(15); SCHED(); half(0, 0); half(0, 1);
        if (s == 0 && wv == 7) { WAITV(0); if (lane == 0) set_flag(ws, FL_G + rb * 8 + cg, tag); }
      }
      BAR(); SCHED();
      const int irow = c * 2 + rg;
      ST(1);
      u32x4 T[2][6]; f32x4 R[7];
#pragma unroll
      for (int t = 0; t < 20; t++) { const int nn = t >> 2, n = nn < 3 ? nn + 2 : nn - 3, m = t & 3;
        const u32x4 w = cat(fb[1][n][0], fb[1][n][1]);
        MFMA_ACC(acc[m][n], fa[1][m], w); SCHED();
        if (t >= 8) {
#pragma unroll
          for (int z = 0; z < 2; z++) { const int i = 2 * (t - 8) + z, tn = i >> 3, tm = (i >> 1) & 3;
            const unsigned v = pk(acc[tm][tn + 2][2 * z], acc[tm][tn + 2][2 * z + 1]);
            T[tm >> 1][tn < 2 ? tn + 2 : 5][2 * (tm & 1) + z] = v; }
          SCHED(); } }
#pragma unroll
      for (int i = 0; i < 8; i++) T[i >> 2][5][i & 3] = r16 < 8 ? 0x3f803f80u : T[i >> 2][5][i & 3];
      SCHED();
      auto rmul = [&](int ks, int i) { constexpr int ta[7] = {0, 1, 0, 1, 4, 4, 4}, tb[7] = {2, 3, 5, 5, 2, 3, 5};
        R[i] = mfma16(T[ks][ta[i]], T[ks][tb[i]], ks ? R[i] : (f32x4){0, 0, 0, 0}); };
#pragma unroll
      for (int ks = 0; ks < 2; ks++) {
        f32x2 u[12], a[12];
#pragma unroll
        for (int i = 0; i < 12; i++) { const int n = i < 4 ? 4 : (i >> 2) - 1, m = 2 * ks + ((i >> 1) & 1);
          u[i] = (f32x2){acc[m][n][2 * (i & 1)], acc[m][n][2 * (i & 1) + 1]}; a[i] = u[i] * 1.44269504088896341f - 1.44269504088896341f; }
        SCHED();
#pragma unroll
        for (int i = 0; i < 12; i++) { if (ks && i < 7) { rmul(0, i); SCHED(); }
          a[i] = (f32x2){__builtin_amdgcn_exp2f(a[i][0]), __builtin_amdgcn_exp2f(a[i][1])}; SCHED(); }
#pragma unroll
        for (int i = 0; i < 12; i++) a[i] = (f32x2){__builtin_amdgcn_fmed3f(u[i][0], a[i][0], 1.f), __builtin_amdgcn_fmed3f(u[i][1], a[i][1], 1.f)};
        SCHED();
#pragma unroll
        for (int i = 0; i < 12; i++) a[i] += 1e-6f;
        SCHED();
        if (ks == 0 && wv == 7) fv = ld_flag(ws, FL_G + r16 * 8 + cg);
        if (ks == 1 && B11) { ST(8); BAR(); ST(9); }
        SCHED();
#pragma unroll
        for (int i = 0; i < 12; i++) { T[ks][i < 4 ? 4 : (i >> 2) - 1][i & 3] = pk(a[i][0], a[i][1]); SCHED();
          if (ks && i >= 5 && i < 8) { rmul(1, i - 1); SCHED(); } if (ks && (i == 9 || i == 10)) { rmul(1, 2 * (10 - i)); SCHED(); } }
      }
      rmul(1, 3); rmul(1, 1);
      SCHED();
      f32x4 M23, M456;
#pragma unroll
      for (int i = 0; i < 4; i++) {
        M23[i] = (q4 & 1) ? R[3][i] : R[2][i];
        const float r45 = (g == 0) ? R[4][i] : R[5][i];
        M456[i] = __uint_as_float(__builtin_amdgcn_permlane32_swap(__float_as_uint(r45), __float_as_uint(R[6][i]), false, false)[0]);
      }
      f32x4 k0, k1;
      const unsigned aR = L + G_RED + lw * 2048 + lane * 16, aR2 = L + G_RED + (lw ^ 2) * 2048 + lane * 16;
      if (rg) { k0 = M23; k1 = M456; st128(aR, 0, __builtin_bit_cast(u32x4, R[0])); st128(aR, 1024, __builtin_bit_cast(u32x4, R[1])); }
      else { k0 = R[0]; k1 = R[1]; st128(aR, 0, __builtin_bit_cast(u32x4, M23)); st128(aR, 1024, __builtin_bit_cast(u32x4, M456)); }
      if (B11) {
        asm volatile("" ::: "memory");
        *(volatile LAS unsigned*)(size_t)(L + L_PAIR + lw * 4) = 1u;
#pragma unroll 1
        for (int it = 0; it < (1 << 22) && !__builtin_amdgcn_readfirstlane(*(volatile LAS unsigned*)(size_t)(L + L_PAIR + (lw ^ 2) * 4)); it++) {}
        asm volatile("" ::: "memory");
      } else { ST(8); WAITL0(); BAR(); ST(9); }
      const bool pset = __all(fv == tag);
      { const f32x4 o0 = __builtin_bit_cast(f32x4, ld128(aR2, 0)), o1 = __builtin_bit_cast(f32x4, ld128(aR2, 1024));
#pragma unroll
        for (int i = 0; i < 4; i++) { k0[i] = add1(k0[i], o0[i]); k1[i] = add1(k1[i], o1[i]); } }
      gst128c(rws, lane * 16 + WS_PART + (cg * 16 + rb) * PRS + irow * 1024, (u32x4){pk(k0[0], k0[1]), pk(k0[2], k0[3]), pk(k1[0], k1[1]), pk(k1[2], k1[3])});
      ST(2);
      if (wv == 7) { if (!pset) { EV(7); wait_flags(ws, FL_G + r16 * 8 + cg, tag); }
        if (lane == 0) __hip_atomic_fetch_add((LAS unsigned*)(size_t)(L + L_DONE), 0x80u, __ATOMIC_RELAXED, __HIP_MEMORY_SCOPE_WORKGROUP); }
      if (CPRIO == 0) __builtin_amdgcn_s_setprio(0);
      ST(10);
      u32x4 pv[4][4];
      const i32x4 so_ = make_srd(Bo, 640 * 640 * 2);
      int gi = lw;
      const unsigned fo = FL_O + rt0 + (lane & 7);
      auto prog = [&]() { return (unsigned)__builtin_amdgcn_readfirstlane(*(volatile LAS unsigned*)(size_t)(L + L_DONE)); };
      auto level = [&]() { return __builtin_popcount(prog() & 0x04040404u); };
      auto wo_groups = [&](int n, int lv) {
#pragma unroll 1
        for (; n > 0 && gi < 40 && gi < 10 * lv + 10; n--, gi += 4) { const int pos = (gi < 10 ? WOP[0] : gi < 20 ? WOP[1] - 30720 : gi < 30 ? WOP[2] - 61440 : WOP[3] - 92160) + gi * 3072;
          dma16x3(so_, vo[0], vo[1], vo[2], (gi + (h1 ? (gi < 20 ? 20 : -20) : 0)) * 16 * 1280 + a3, L + pos); } };
      wo_groups(10, 0);
      const unsigned pb = WS_PART + cg * 16 * PRS + irow * 1024, gr = (rb >> 2) ^ 3;
      const i32x4 sws = make_srd(ws, WS_BYTES);
#pragma unroll 1
      for (int it = 0; it < (1 << 20) && (prog() & (PRE ? 0x84u : 0x80u)) != (PRE ? 0x84u : 0x80u); it++) __builtin_amdgcn_s_sleep(1);
      asm volatile("" ::: "memory");
      if (PRE == 1) wo_groups(10, 1);
      if (GD) __builtin_amdgcn_s_sleep(GD);
      ST(13);
      f32x4 sum[2];
      auto reduce = [&]() {
#pragma unroll
        for (int i = 0; i < 8; i++)
#pragma unroll
          for (int bp = 0; bp < 2; bp++) { sum[bp] = mfma16(sel[i & 1], pv[i >> 1][2 * bp + (i & 1)], i ? sum[bp] : (f32x4){0, 0, 0, 0}); SCHED(); } };
#pragma unroll
      for (int i = 0; i < 16; i++) { pv[i >> 2][i & 3] = gld128c(rws, pvo + (i & 3) * 256, pb + (gr ^ (i >> 2)) * 4 * PRS); SCHED(); }
      if (wv == 7) fv = ld_flag(ws, fo);
      SCHED();
      reduce();
      const bool oset = __all(fv == tag);
      SCHED();
      wo_groups(PG, level());
      if (__builtin_amdgcn_ballot_w64(__builtin_isunordered(sum[0][0], sum[1][0]))) {
      EV(0);
      unsigned pend = 15;
#pragma unroll 1
      for (int it = 0; it < SWEEPS; it++) {
#pragma unroll
        for (int rp = 0; rp < 4; rp++) if ((pend >> rp & 1) && none_empty(mx4(mx4(mx4(mx4(0, pv[rp][0]), pv[rp][1]), pv[rp][2]), pv[rp][3]))) pend &= ~(1u << rp);
        if (!pend) break;
        EV(1);
#define GRP(r, s, a, b, c, d) "s_bitcmp1_b32 %18, " #r "\n\ts_cbranch_scc0 " #r "f\n\tbuffer_load_dwordx4 %" #a ", %16, %17, %" #s " offen sc1\n\tbuffer_load_dwordx4 %" #b ", %16, %17, %" #s " offen offset:256 sc1\n\t" \
                              "buffer_load_dwordx4 %" #c ", %16, %17, %" #s " offen offset:512 sc1\n\tbuffer_load_dwordx4 %" #d ", %16, %17, %" #s " offen offset:768 sc1\n" #r ":\n\t"
        asm volatile("s_nop 4\n\t" GRP(0, 19, 0, 1, 2, 3) GRP(1, 20, 4, 5, 6, 7) GRP(2, 21, 8, 9, 10, 11) GRP(3, 22, 12, 13, 14, 15) "s_waitcnt vmcnt(0)"
                     : "+v"(pv[0][0]), "+v"(pv[0][1]), "+v"(pv[0][2]), "+v"(pv[0][3]), "+v"(pv[1][0]), "+v"(pv[1][1]), "+v"(pv[1][2]), "+v"(pv[1][3]),
                       "+v"(pv[2][0]), "+v"(pv[2][1]), "+v"(pv[2][2]), "+v"(pv[2][3]), "+v"(pv[3][0]), "+v"(pv[3][1]), "+v"(pv[3][2]), "+v"(pv[3][3])
                     : "v"(pvo), "s"(sws), "s"(pend), "s"(pb + gr * 4 * PRS), "s"(pb + (gr ^ 1) * 4 * PRS), "s"(pb + (gr ^ 2) * 4 * PRS), "s"(pb + (gr ^ 3) * 4 * PRS) : "scc", "memory");
#undef GRP
      }
      reduce();
      }
      ST(3);
      {
#pragma unroll
        for (int bp = 0; bp < 2; bp++) st64(aS, bp * 512, (u32x2){pk(sum[bp][0], sum[bp][1]), pk(sum[bp][2], sum[bp][3])}); }
      auto o_ok = [&]() { if (lane == 0) __hip_atomic_fetch_add((LAS unsigned*)(size_t)(L + L_DONE), 0x8000u, __ATOMIC_RELAXED, __HIP_MEMORY_SCOPE_WORKGROUP); };
      if (wv == 7 && oset) o_ok();
      ST(11);
#pragma unroll 1
      for (int it = 0; it < (1 << 20) && gi < 40; it++) { const int lv = level(); if (lv == 4) break; if (gi < 10 * lv + 10) wo_groups(1, lv); else __builtin_amdgcn_s_sleep(2); }
      WAITL0(); BAR();
      ST(4);
      if (!oset) { EV(2); wait_flags(ws, fo, tag); o_ok(); }
      __builtin_amdgcn_s_setprio(0);
      wo_groups(10, 3);
      ST(12);
      const unsigned so = WS_O + (rt0 + 2 * lw) * OS, sc = L + (lw < 3 ? G_OUT + lw * 5120 : 30720), sp_[2] = {so + h1 * 10240, so + (h1 ^ 1) * 10240};
      unsigned lo[2] = {lane * 16u, lane * 16u + OS}, sw_ = sc + r16 * 160 + g * 8, sr = sc + lane * 16, lko = (((lane >> 1) & 3) ^ (2 * h1)) * 5120 + (lane & 1) * OS;
      PIN(sw_); PIN(sr); PIN(lo[0]); PIN(lo[1]); PIN(lko);
      WAITV(0); arrive(); wait_cnt(8);
      if (SD) __builtin_amdgcn_s_sleep(SD);
      f32x4 oa[2][5];
#pragma unroll 1
      for (int pass = 0; pass < 2; pass++) {
        u32x2 wf[3][5][2]; u32x4 of[6][2];
        auto look = [&]() { asm volatile("" ::: "memory"); return __builtin_amdgcn_raw_buffer_load_b32(rws, lko, so + 4096 + 1020, SC1); };
        auto await = [&](unsigned mask) {
#pragma unroll 1
          for (int it = 0; it < SWEEPS && (__builtin_amdgcn_ballot_w64(look() == EMPTY) & mask); it++) {}
          asm volatile("" ::: "memory"); };
        if (pass) { EV(5);
#pragma unroll 1
          for (int it = 0; it < SWEEPS; it++) { unsigned m = 0; asm volatile("" ::: "memory");
#pragma unroll 4
            for (int i = 0; i < 20; i++) m = mx4(mx4(m, gld128c(rws, lo[0], so + i * 1024)), gld128c(rws, lo[1], so + i * 1024));
            if (none_empty(m)) break; }
        }
        asm volatile("" ::: "memory");
        ST(5);
        auto rdo = [&](int ks, int n, int h) { const int p = ks / 5, f = ks % 5;
          wf[ks % 3][n][h] = ldtr(aW[(p + 1) >> 1][f < 4 ? h : 2][n & 1], (p == 2 ? WOP[2] - WOP[1] : 0) + (f < 4 ? f * 40 + 20 * h : 80 * h) * 192 + (n >> 1) * 64); };
        auto ldo = [&](int ks, int m) { of[ks % 6][m] = gld128c(rws, lo[m] + (ks % 10 & 3) * 1024, sp_[ks / 10] + (ks % 10 >> 2) * 4096); };
        unsigned lk;
#pragma unroll 1
        for (int it = 0; it < SWEEPS; it++) {
          lk = look();
          SCHED();
#pragma unroll
          for (int ks = 0; ks < 5; ks++) { ldo(ks, 0); SCHED(); ldo(ks, 1); SCHED(); }
#pragma unroll
          for (int i = 0; i < 10; i++) { rdo(i / 5, i % 5, 0); rdo(i / 5, i % 5, 1); }
          SCHED();
          if (!(__builtin_amdgcn_ballot_w64(lk == EMPTY) & 0xf)) break;
          EV(3);
          if (it >= RETRY) await(0xf);
        }
        SCHED();
#pragma unroll
        for (int ks = 0; ks < 20; ks++) {
          if (ks == 1) lk = look();
          if (ks == 5 && (__builtin_amdgcn_ballot_w64(lk == EMPTY) & 0xf0)) { EV(4); await(0xf0); }
#pragma unroll
          for (int n = 0; n < 5; n++) {
            if (n == 0) {
              if (ks >= 1 && ks < 5) __builtin_amdgcn_s_waitcnt(0x0a79); else if (ks < 16) __builtin_amdgcn_s_waitcnt(0x0a78); else if (ks == 16) __builtin_amdgcn_s_waitcnt(0x0a76);
              else if (ks == 17) __builtin_amdgcn_s_waitcnt(0x0a74); else if (ks == 18) __builtin_amdgcn_s_waitcnt(0x0a72); else __builtin_amdgcn_s_waitcnt(0x0070); }
            if (ks % 5 == 4 && n == 0) {
              const u32x4 e = of[ks % 6][0], o = of[ks % 6][1];
              of[ks % 6][0] = (u32x4){e[0], e[1], o[0], o[1]}; of[ks % 6][1] = (u32x4){e[2], e[3], o[2], o[3]};
              asm volatile("s_nop 1" : "+v"(of[ks % 6][0]), "+v"(of[ks % 6][1])); }
            if (ks < 15 && (n == 1 || n == 4)) ldo(ks + 5, n >> 2);
            SCHED();
            const u32x4 w = cat(wf[ks % 3][n][0], wf[ks % 3][n][1]);
            if (ks == 0) MFMA_NEW(oa[0][n], w, of[ks % 6][0]); else MFMA_ACC(oa[0][n], w, of[ks % 6][0]);
            SCHED();
            if (ks < 18) rdo(ks + 2, n, 0);
            SCHED();
            if (ks == 0) MFMA_NEW(oa[1][n], w, of[ks % 6][1]); else MFMA_ACC(oa[1][n], w, of[ks % 6][1]);
            SCHED();
            if (ks < 18) rdo(ks + 2, n, 1);
            SCHED();
          }
        }
        asm volatile("s_nop 7" : "+v"(oa[0][0]), "+v"(oa[0][1]), "+v"(oa[0][2]), "+v"(oa[0][3]), "+v"(oa[0][4]), "+v"(oa[1][0]), "+v"(oa[1][1]), "+v"(oa[1][2]), "+v"(oa[1][3]), "+v"(oa[1][4]));
        if (__builtin_amdgcn_ballot_w64(__builtin_isunordered(oa[0][0][0], oa[1][0][0])) == 0) break;
      }
      ST(6);
#pragma unroll
      for (int m = 0; m < 2; m++)
#pragma unroll
        for (int n = 0; n < 5; n++) { st64(sw_, m * 2560 + n * 32, (u32x2){pk(oa[m][n][0], oa[m][n][1]), pk(oa[m][n][2], oa[m][n][3])}); SCHED(); }
      { u32x4 r[5];
#pragma unroll
        for (int u = 0; u < 5; u++) r[u] = ld128(sr, u * 1024);
#pragma unroll
        for (int u = 0; u < 5; u++) __builtin_amdgcn_raw_buffer_store_b128(r[u], ro, go[u], 0, OUTB); }
    }
  } else {
    const int ch0 = 320 * hf + 5 * ai;
    const rsrc_t rh = mk(Ah, 640 * 4096);
    u32x4 idl, idh, dm, bw[3][2];
    { const unsigned hm = (g & 1) == (r16 >> 3) ? 0xffffu << ((r16 & 1) * 16) : 0u, one = g < 2 ? 0x3f803f80u : 0u;
#pragma unroll
      for (int i = 0; i < 4; i++) { dm[i] = ((r16 >> 1) & 3) == i ? hm : 0u; idl[i] = dm[i] & one; idh[i] = dm[i] & (one ^ 0x3f803f80u); } }
    unsigned aK[5];
#pragma unroll
    for (int i = 0; i < 5; i++) { const int row = 8 * q4 + 3 - p4 + (i < 4 ? 4 + 4 * (i >> 1) - 2 * (i & 1) - (g >> 1) : 4 * (g >> 1));
      aK[i] = L + (i < 4 ? A_K : A_V) + row * 256 + ((((row >> 1) & 12) ^ (row & 3) ^ (g & 1) ^ (2 * wv)) << 4); PIN(aK[i]); }
    unsigned ko = WS_KVT + hf * 256 * KS + (255 - 4 * ai - g) * KS + r16 * 16 + wv * 256, hpo = (tid & 255) ? ch0 * 4096 + tid * 16 - 8 : 0xffff0000u;
    PIN(ko); PIN(hpo);
    if (wv == 7) { set_warm(); PIN(va[0]); PIN(va[1]); }
    const int chl = wv < 4 ? wv : 4;
    const float Dc = __uint_as_float((*(const __attribute__((address_space(4))) unsigned*)(AD + ((ch0 + chl) & ~1)) >> (((ch0 + chl) & 1) * 16)) << 16);
    if (tid < 6) *(LAS unsigned*)(size_t)(L + L_DONE + tid * 4) = 0;
    asm volatile("s_waitcnt vmcnt(6)" : "+v"(wk), "+v"(dm), "+v"(idl), "+v"(idh)::"memory");
    BAR();
    u32x4 hv[3]; u32x2 hp[3];
    auto load_h = [&]() {
#pragma unroll
      for (int u = 0; u < 3; u++) if (u < 2 || wv < 4) {
        hv[u] = gld128(rh, tid * 16, ch0 * 4096 + u * 8192);
        hp[u] = __builtin_amdgcn_raw_buffer_load_b64(rh, hpo, u * 8192, 0); } };
    auto warm_wo = [&]() { const i32x4 so_ = make_srd(Bo, 640 * 640 * 2);
      const unsigned r = (lane * 52) >> 8, vq = (r * 1280 + (lane - 5 * r) * 128) | (lane < 50 ? 0u : 0xf0000000u);
#pragma unroll
      for (int i = 0; i < 4; i++) touch(so_, vq, (i * 160 + 10 * j) * 1280 + (xcd & 1) * 640, L + L_DUMMY); };
    auto warm_wq = [&]() { const unsigned r = (lane * 52) >> 8, vq = (r * 3840 + (lane - 5 * r) * 128) | (lane < 50 ? 0u : 0xf0000000u);
#pragma unroll
      for (int i = 0; i < 4; i++) touch(sw, vq, (i * 160 + 10 * j) * 3840 + (xcd & 1) * 640, L + L_DUMMY); };
    if (wv == 7) {
#pragma unroll
      for (int s = P1 + 1; s < 10; s++) warm(s);
      if (WQB == 0) warm_wq();
    }
#pragma unroll
    for (int u = 0; u < 3; u++)
#pragma unroll
      for (int e = 0; e < 2; e++) { const unsigned w = __builtin_amdgcn_ds_bpermute((16 * u + r16) * 4, wk[e]);
        bw[u][e] = dm & __builtin_amdgcn_perm(w, w, g < 2 ? 0x01000100u : 0x03020302u); PIN(bw[u][e]); }
    ST(14);
    if (wv == 7) asm volatile("s_waitcnt vmcnt(%0)" ::"i"((WQB == 0 ? 22 : 18) - 2 * P1) : "memory"); else WAITV(0);
    ST(15); BAR();
    ST(1);
    auto kvt = [&](int nt) {
      u32x4 fr[3][5]; f32x4 k0[3], k1[3], v0[3], v1[3];
#pragma unroll
      for (int u = 0; u < nt; u++)
#pragma unroll
        for (int i = 0; i < 5; i++) { fr[u][i] = ld128(aK[i], (i < 4 ? KP : VP) * u); SCHED(); }
      if (HB == 1) load_h();
      SCHED();
#pragma unroll
      for (int u = 0; u <= nt; u++) {
        if (u < nt) { const f32x4 z = {0, 0, 0, 0};
          k0[u] = mfma16(fr[u][0], bw[u][0], z); SCHED(); k1[u] = mfma16(fr[u][2], bw[u][0], z); SCHED();
          k0[u] = mfma16(fr[u][1], bw[u][1], k0[u]); SCHED(); k1[u] = mfma16(fr[u][3], bw[u][1], k1[u]); SCHED();
          v0[u] = mfma16(fr[u][4], idl, z); SCHED(); v1[u] = mfma16(fr[u][4], idh, z); SCHED(); }
        if (u > 0) { const int w = u - 1;
          gst128c(rws, ko + w * 2048, (u32x4){pk(mul1(k1[w][0], v1[w][0]), mul1(k1[w][1], v1[w][1])), pk(mul1(k1[w][2], v1[w][2]), mul1(k1[w][3], v1[w][3])),
                                              pk(mul1(k0[w][0], v0[w][0]), mul1(k0[w][1], v0[w][1])), pk(mul1(k0[w][2], v0[w][2]), mul1(k0[w][3], v0[w][3]))}); SCHED(); }
      } };
    if (wv < 4) kvt(3); else kvt(2);
    ST(8);
    unsigned xo, xa, ha = L + (tid >> 8) * HCH + 384 + (tid & 255) * 16;
    { const unsigned blk = (tid * 13108) >> 16, ch = tid - 5 * blk; xo = blk * KS + ch * 16; xa = L + A_X + ch * XCH + blk * 16; }
    unsigned aH = L + lw * HCH + (3 - p4) * HCP + (64 * q4 + 8 * g) * 2;
    const int bc = 8 * (r16 & 7) + 4 * (r16 >> 3);
    unsigned aXr = L + A_X + lw * XCH + (1852 - 1792 - bc + 8 * g) * 2;
    unsigned aE = L + A_X + chl * XCH + (2044 - 1792 - 64 * g - bc) * 2;
    unsigned aH5 = aH + A_H4 - lw * HCH + 128 * lw, aX5 = aXr + (4 - lw) * XCH + 128 * lw, aD = L + A_DMP + lw * 2048 + lane * 16, aF = L + L_DONE + lw * 4, aC = L + L_CNT;
    unsigned qo = (tid >> 4) * 1280 + r16 * 16;
    PIN(xo); PIN(xa); PIN(ha); PIN(aH); PIN(aXr); PIN(aE); PIN(aH5); PIN(aX5); PIN(aD); PIN(aF); PIN(aC); PIN(qo);
    auto hcopy = [&](int u0, int u1) {
#pragma unroll
      for (int u = u0; u < u1; u++) if (u < 2 || wv < 4) {
        const unsigned e0 = __builtin_amdgcn_alignbyte(hp[u][1], hp[u][0], 2), e1 = __builtin_amdgcn_alignbyte(hv[u][0], hp[u][1], 2), e2 = __builtin_amdgcn_alignbyte(hv[u][1], hv[u][0], 2),
                       e3 = __builtin_amdgcn_alignbyte(hv[u][2], hv[u][1], 2), e4 = __builtin_amdgcn_alignbyte(hv[u][3], hv[u][2], 2);
        const unsigned ad = u < 2 ? ha : ha + A_H4; const int o = u == 1 ? 2 * HCH : 0;
        st128(ad, o, hv[u]); st128(ad, o + HCP, (u32x4){e1, e2, e3, e4}); st128(ad, o + 2 * HCP, (u32x4){hp[u][1], hv[u][0], hv[u][1], hv[u][2]}); st128(ad, o + 3 * HCP, (u32x4){e0, e1, e2, e3}); } };
    WAITV(0);
    BAR();
    if (wv == 7 && lane == 0) set_flag(ws, FL_K + hf * 64 + ai, tag);
    ST(2);
    auto reset_o = [&]() { if (wv == 5 || wv == 6) { const unsigned c2 = WS_O + (hf * 64 + ai) * OS + (wv - 5) * 10240 + lane * 16;
#pragma unroll
        for (int i = 0; i < 10; i++) gst128c(rws, c2 + i * 1024, emp); } };
    if (OCL == 0) reset_o();
    if (HB == 0) load_h();
    if (HB == 1 && wv < 7) hcopy(0, 3);
    if (wv == 6) { if (WQB == 1) warm_wq(); if (WOT == 0) warm_wo(); }
    if (OCL == 1) reset_o();
    if ((tid & 31) < 24) { st128(L + (tid >> 5) * HCP + (tid & 31) * 16, 0, (u32x4){0, 0, 0, 0}); if (wv < 2) st128(L + A_H4 + (tid >> 5) * HCP + (tid & 31) * 16, 0, (u32x4){0, 0, 0, 0}); }
    else if (tid < 160) st128(L + A_X + (tid >> 5) * XCH + 4096 + (tid & 7) * 16, 0, (u32x4){0, 0, 0, 0});
    if (wv == 7) wait_flags(ws, FL_K + hf * 64 + lane, tag);
    ST(9);
    WAITL0(); BAR();
    ST(3);
    u32x4 F[2][8];
    if (wv < 5) { u32x4 xv[4]; const int xr = ROTO ? ai & 3 : 0;
#pragma unroll
      for (int u = 0; u < 4; u++) xv[u] = gld128c(rws, xo, WS_KVT + hf * 256 * KS + ai * 80 + (u ^ xr) * 64 * KS);
      SCHED();
      if (HB == 0) hcopy(0, 3);
      else if (wv < 4)
#pragma unroll
        for (int t = 0; t < 8; t++) F[0][t] = ld128(aH5, 512 * t);
      SCHED();
#pragma unroll
      for (int u = 0; u < 4; u++) st128(xa + (u ^ xr) * 1024, 0, xv[u]);
    } else {
      if (HB == 0 || wv == 7) hcopy(0, 3);
      if (OCL == 2) reset_o();
      if (WOT == 1 && wv == 7) warm_wo();
    }
    auto resets = [&]() { const unsigned c1 = WS_YT + (ch0 + lw) * YS + lane * 16, c3 = WS_YT + (ch0 + 4) * YS + lw * 1024 + lane * 16;
#pragma unroll
      for (int i = 0; i < 5; i++) gst128c(rws, i < 4 ? c1 + i * 1024 : c3, emp); };
    if (OCL < 3 && (wv == 5 || wv == 6)) WAITV(0);
    const rsrc_t rq = mk(Aq, 2048 * 1280);
    u32x4 qv[3];
    unsigned qo2 = (qo + 512) | (r16 & 8) << 28; PIN(qo2);
    auto load_q = [&]() {
#pragma unroll
      for (int u = 0; u < 3; u++) qv[u] = gld128(rq, u < 2 ? qo + u * 256 : qo2, 32 * ai * 1280 + hf * 640); };
    unsigned yo = WS_YT + (ch0 + chl) * YS + 128 * g + (r16 & 7) * 16 + (r16 >> 3) * 512; PIN(yo);
    auto put_y = [&](f32x4 a, f32x4 c2, unsigned off) {
      const unsigned a0_ = pk(a[0], a[1]), a1_ = pk(a[2], a[3]), b0 = pk(c2[0], c2[1]), b1 = pk(c2[2], c2[3]);
      gst128c(rws, yo + off, (u32x4){(unsigned)__builtin_amdgcn_update_dpp(a0_, b0, 0x128, 0xf, 0xc, false), (unsigned)__builtin_amdgcn_update_dpp(a1_, b1, 0x128, 0xf, 0xc, false),
                                     (unsigned)__builtin_amdgcn_update_dpp(b0, a0_, 0x128, 0xf, 0x3, false), (unsigned)__builtin_amdgcn_update_dpp(b1, a1_, 0x128, 0xf, 0x3, false)}); };
    WAITL0(); BAR();
    ST(4);
    if (wv < 4) {
      f32x4 acc[8], ac5[8];
      u32x2 G[2][8][2], xe[8];
      unsigned cv; f32x4 xs[8];
#pragma unroll
      for (int t = 0; t < 8; t++) { if (HB == 0) F[0][t] = ld128(aH5, 512 * t); G[0][t][0] = ld64(aX5, 512 * (7 - t)); SCHED(); G[0][t][1] = ld64(aX5, 512 * (7 - t) + 8); SCHED(); }
      auto step = [&](f32x4* ac, int cur, unsigned nh, unsigned nx, int off, int mode) {
        int cnt = 0;
#pragma unroll
        for (int t = 0; t < 8; t++)
#pragma unroll
          for (int p = 0; p <= t; p++) {
            const u32x4 gg = cat(G[cur][t - p][0], G[cur][t - p][1]);
            if (p == 0 && (mode & 1)) MFMA_NEW(ac[t], F[cur][p], gg); else MFMA_ACC(ac[t], F[cur][p], gg);
            SCHED();
            if (cnt < 24 && !(mode & 32)) { const int i = cnt / 3, w = cnt % 3;
              if (w == 0) F[1 - cur][i] = ld128(nh, 512 * i + off); else G[1 - cur][i][w - 1] = ld64(nx, 512 * (7 - i) + off + 8 * (w - 1)); }
            if ((mode & 6) && cnt >= 24 && cnt % 3 == 0) { const int d = (cnt - 24) / 3 + (mode & 4); st128(aD, (d >> 1) * 8192 + (d & 1) * 1024, __builtin_bit_cast(u32x4, ac5[d])); }
            if ((mode & 4) && cnt == 35) *(volatile LAS unsigned*)(size_t)aF = 1u;
            if ((mode & 8) && cnt == 25) { aH += 128; PIN(aH); }
            if ((mode & 8) && cnt == 26) { aXr += 128; PIN(aXr); }
            if ((mode & 16) && cnt >= 24 && cnt < 32) xe[cnt - 24] = ld64(aE, 512 * (31 - cnt));
            if ((mode & 16) && cnt == 32) cv = *(volatile LAS unsigned*)(size_t)aC;
            if (mode & 32) {
              const int e = cnt & 3, tt = cnt >> 2;
              if (cnt < 32) { unsigned v = e & 1 ? xe[tt][1 - (e >> 1)] << 16 : xe[tt][1 - (e >> 1)] & 0xffff0000u; PIN(v); xs[tt][e] = __uint_as_float(v); } }
            if (cnt == 34 && !(mode & 32)) { if (mode & 6) WAITL(4); else WAITL0(); }
            cnt++;
            SCHED();
          }
      };
#define STC(n)
      step(ac5, 0, aH5, aX5, 64, 1); STC(13); step(ac5, 1, aH, aXr, 0, 0); STC(14);
      step(acc, 0, aH, aXr, 64, 1 | 2); STC(15); step(acc, 1, aH, aXr, 128, 4 | 8); STC(8);
      load_q(); SCHED();
#pragma unroll 1
      for (int r = 0; r < 2; r++) { step(acc, 0, aH, aXr, 64, 0); step(acc, 1, aH, aXr, 128, 8); }
      step(acc, 0, aH, aXr, 64, 16); step(acc, 1, aH, aXr, 0, 32);
      ST(10);
      if (__builtin_amdgcn_readfirstlane(cv) < 8) wait_cnt(8);
#pragma unroll
      for (int t = 0; t < 8; t += 2) { if (t == 6) asm volatile("" : "+v"(acc[6]), "+v"(acc[7]));
        put_y(acc[t] + Dc * xs[t], acc[t + 1] + Dc * xs[t + 1], 512 * t); SCHED(); }
    } else {
      __builtin_amdgcn_s_setprio(HPRIO);
      if (OCL == 3) { const unsigned c2 = WS_O + (hf * 64 + ai) * OS + lw * 5120 + lane * 16;
#pragma unroll
        for (int i = 0; i < 5; i++) gst128c(rws, c2 + i * 1024, emp);
        WAITV(0); }
      arrive(); wait_cnt(4);
      if (wv == 7 && lane == 0) set_flag(ws, FL_O + hf * 64 + ai, tag);
      resets();
      WAITV(0); arrive(); wait_cnt(8);
      if (wv == 7 && lane == 0) set_flag(ws, FL_A + hf * 64 + ai, tag);
#pragma unroll 1
      for (int it = 0; it < (1 << 20) && !__all(*(volatile LAS unsigned*)(size_t)(L + L_DONE + p4 * 4) == 1u); it++) __builtin_amdgcn_s_sleep(2);
      asm volatile("" ::: "memory");
      { const unsigned rb_ = L + A_DMP + lw * 8192 + lane * 16;
        f32x4 sm[2];
#pragma unroll
        for (int e = 0; e < 2; e++) { sm[e] = __builtin_bit_cast(f32x4, ld128(rb_, e * 1024));
#pragma unroll
          for (int w = 1; w < 4; w++) { const f32x4 o = __builtin_bit_cast(f32x4, ld128(rb_, w * 2048 + e * 1024));
#pragma unroll
            for (int i = 0; i < 4; i++) sm[e][i] = add1(sm[e][i], o[i]); }
          const u32x2 xr = ld64(aE + 512 * (7 - 2 * lw - e), 0);
          sm[e] = (f32x4){fma1(Dc, bhi(xr[1]), sm[e][0]), fma1(Dc, blo(xr[1]), sm[e][1]), fma1(Dc, bhi(xr[0]), sm[e][2]), fma1(Dc, blo(xr[0]), sm[e][3])}; }
        put_y(sm[0], sm[1], 1024 * lw); }
      load_q();
      if (wv == 7) {
#pragma unroll 1
        for (int it = 0; it < (1 << 18); it++) {
          bool ok = ld_flag(ws, FL_A + hf * 64 + lane) == tag;
          if (b == 128) ok = ok & (ld_flag(ws, lane) == tag) & (ld_flag(ws, 64 + lane) == tag) & (ld_flag(ws, 128 + lane) == tag) & (ld_flag(ws, 192 + lane) == tag);
          if (__all(ok)) break;
          __builtin_amdgcn_s_sleep(4);
        }
        asm volatile("" ::: "memory");
        if (b == 128 && lane == 0) __hip_atomic_store((GAS unsigned*)(ws + WS_EPOCH), tag, __ATOMIC_RELAXED, __HIP_MEMORY_SCOPE_AGENT);
      }
      __builtin_amdgcn_s_setprio(0);
    }
    ST(5);
    WAITV(0);
    WAITL0(); BAR();
    if (YD > 0) __builtin_amdgcn_s_sleep(YD > 0 ? YD : 0);
    ST(11);
    { u32x4 yv[3];
      const unsigned yl = (tid >> 2) * YS + p4 * 16, ys = L + T_SCR + p4 * (8 * 640 + 32) + (tid >> 2) * 2;
      yv[2] = (u32x4){0, 0, 0, 0};
#pragma unroll
      for (int u = 0; u < 3; u++) if (u < 2 || wv < 4) yv[u] = gld128c(rws, yl, WS_YT + (320 * hf + 128 * u) * YS + ai * 64);
#pragma unroll 1
      for (int it = 0; it < SWEEPS && !none_empty(mx4(mx4(mx4(0, yv[0]), yv[1]), yv[2])); it++) {
        EV(6);
        asm volatile("" ::: "memory");
#pragma unroll
        for (int u = 0; u < 3; u++) if (mx4(0, yv[u]) == EMPTY) yv[u] = gld128c(rws, yl, WS_YT + (320 * hf + 128 * u) * YS + ai * 64);
      }
      ST(6);
#pragma unroll
      for (int u = 0; u < 3; u++) if (u < 2 || wv < 4) {
#pragma unroll
        for (int e = 0; e < 4; e++) { st16(ys, 256 * u + 1280 * e, (u16)yv[u][e]); st16(ys, 256 * u + 1280 * e + 640, (u16)(yv[u][e] >> 16)); } } }
    WAITL0(); BAR();
    ST(12);
    { const unsigned ya = L + T_SCR + (tid >> 4) * 640 + (wv >> 1) * 32 + r16 * 16, oo = (tid >> 4) * 2560 + r16 * 16 + 32 * ai * 2560 + hf * 640;
      u32x4 y[3];
#pragma unroll
      for (int u = 0; u < 3; u++) y[u] = ld128(ya, 256 * u);
      SCHED();
#pragma unroll
      for (int u = 0; u < 3; u++) { u32x4 r;
#pragma unroll
        for (int e = 0; e < 4; e++) r[e] = pk(blo(y[u][e]) * blo(qv[u][e]), bhi(y[u][e]) * bhi(qv[u][e]));
        __builtin_amdgcn_raw_buffer_store_b128(r, ro, u < 2 ? oo + u * 256 : (oo + 512) | (r16 & 8) << 28, 0, OUTA); } }
  }
  ST(7); STEND();
}

extern "C" void kernel_launch(void* const* d_in, const int* in_sizes, int n_in, void* d_out, int out_size, void* d_ws, size_t ws_size, hipStream_t stream) {
  fused_kernel<<<256, 512, 0, stream>>>((const u16*)d_in[0], (const u16*)d_in[1], (const u16*)d_in[2], (const u16*)d_in[3], (const u16*)d_in[4], (const u16*)d_in[5],
                                        (const u16*)d_in[6], (const u16*)d_in[7], (const u16*)d_in[8], (u16*)d_out, (char*)d_ws);
}
```

```cpp
#include <hip/hip_runtime.h>
#include <hip/hip_bf16.h>

typedef unsigned short u16;
typedef unsigned long long u64;
typedef short s16x4 __attribute__((ext_vector_type(4)));
typedef short s16x8 __attribute__((ext_vector_type(8)));
typedef __bf16 bf16x2 __attribute__((ext_vector_type(2)));
typedef __bf16 bf16x8 __attribute__((ext_vector_type(8)));
typedef float f32x2 __attribute__((ext_vector_type(2)));
typedef float f32x4 __attribute__((ext_vector_type(4)));
typedef unsigned u32x2 __attribute__((ext_vector_type(2)));
typedef unsigned u32x4 __attribute__((ext_vector_type(4)));
typedef int i32x4 __attribute__((ext_vector_type(4)));
#define LAS __attribute__((address_space(3)))
#define GAS __attribute__((address_space(1)))
#define DI __device__ __forceinline__
#define WAITV(n) asm volatile("s_waitcnt vmcnt(" #n ")" ::: "memory")
#define WAITL0() __builtin_amdgcn_s_waitcnt(0xC07F)
#define WAITL(n) __builtin_amdgcn_s_waitcnt(0xC07F | ((n) << 8))
#define BAR() do { asm volatile("" ::: "memory"); __builtin_amdgcn_s_barrier(); asm volatile("" ::: "memory"); } while (0)
#define SCHED() __builtin_amdgcn_sched_barrier(0)
#define P1 6
#define WR 15
#define CLD 0
#define WOT 0
#define ROTO 1
#define P0 0
#define HB 0
#define WQB 1
#define YD 0
#define GR 0
#define WO0 0
#define EARLY 1
#define RETRY 100
#define PG 0
#define OCL 2
#define GD 0
#define B11 1
#define HPRIO 1
#define CLE 0
#define SPEC 2
#define SD 0
#define CPRIO 1
#define PRE 1
#define LEAP 1
#define OUTA 16
#define OUTB 0
#define CODEB 35072
#define STAMP 1

DI f32x4 mfma16(u32x4 a, u32x4 b, f32x4 c) { return __builtin_amdgcn_mfma_f32_16x16x32_bf16(__builtin_bit_cast(bf16x8, a), __builtin_bit_cast(bf16x8, b), c, 0, 0, 0); }
DI u32x4 cat(u32x2 a, u32x2 b) { return __builtin_shufflevector(a, b, 0, 1, 2, 3); }
DI u32x4 ld128(unsigned a, int off) { return *(const LAS u32x4*)((LAS char*)(size_t)a + off); }
DI u32x2 ld64(unsigned a, int off) { return *(const LAS u32x2*)((LAS char*)(size_t)a + off); }
DI u32x2 ldtr(unsigned a, int off) { return __builtin_bit_cast(u32x2, __builtin_amdgcn_ds_read_tr16_b64_v4i16((LAS s16x4*)((LAS char*)(size_t)a + off))); }
DI void st128(unsigned a, int off, u32x4 v) { *(LAS u32x4*)((LAS char*)(size_t)a + off) = v; }
DI void st64(unsigned a, int off, u32x2 v) { *(LAS u32x2*)((LAS char*)(size_t)a + off) = v; }
DI void st16(unsigned a, int off, u16 v) { *(LAS u16*)((LAS char*)(size_t)a + off) = v; }
DI unsigned pk(float lo, float hi) { f32x2 v = {lo, hi}; return __builtin_bit_cast(unsigned, __builtin_convertvector(v, bf16x2)); }
DI float blo(unsigned u) { return __uint_as_float(u << 16); }
DI float bhi(unsigned u) { return __uint_as_float(u & 0xffff0000u); }
DI unsigned phi2(float u0, float u1) {
  const f32x2 u = {u0, u1}, a = u * 1.44269504088896341f - 1.44269504088896341f;
  f32x2 p = {__builtin_amdgcn_fmed3f(u0, __builtin_amdgcn_exp2f(a[0]), 1.f), __builtin_amdgcn_fmed3f(u1, __builtin_amdgcn_exp2f(a[1]), 1.f)};
  p += 1e-6f;
  return pk(p[0], p[1]);
}
DI float add1(float a, float b) { float r = a + b; asm("" : "+v"(r)); return r; }
DI float mul1(float a, float b) { float r = a * b; asm("" : "+v"(r)); return r; }
DI float fma1(float a, float b, float c) { float r = __builtin_fmaf(a, b, c); asm("" : "+v"(r)); return r; }
#define MFMA_NEW(acc, a, b) asm("v_mfma_f32_16x16x32_bf16 %0, %1, %2, 0" : "=&v"(acc) : "v"(a), "v"(b))
#define MFMA_ONE(acc, a, b) asm("v_mfma_f32_16x16x32_bf16 %0, %1, %2, 1.0" : "=&v"(acc) : "v"(a), "v"(b))
#define MFMA_INI(acc, a, b, c) asm("v_mfma_f32_16x16x32_bf16 %0, %1, %2, %3" : "=&v"(acc) : "v"(a), "v"(b), "v"(c))
#define MFMA_ACC(acc, a, b) asm("v_mfma_f32_16x16x32_bf16 %0, %1, %2, %0" : "+v"(acc) : "v"(a), "v"(b))
DI i32x4 make_srd(const void* p, unsigned bytes) { u64 a = (u64)p; i32x4 r; r[0] = (int)(unsigned)a; r[1] = (int)(unsigned)(a >> 32) & 0xffff; r[2] = (int)bytes; r[3] = 0x00020000; return r; }
DI void dma16(i32x4 srd, unsigned voff, unsigned soff, unsigned m0v) {
  asm volatile("s_mov_b32 m0, %3\n\ts_nop 0\n\tbuffer_load_dwordx4 %0, %1, %2 offen lds" ::"v"(voff), "s"(srd), "s"(soff), "s"(m0v) : "memory");
}
DI void dma16x3(i32x4 srd, unsigned v0, unsigned v1, unsigned v2, unsigned soff, unsigned m0v) {
  asm volatile("s_mov_b32 m0, %5\n\ts_nop 0\n\tbuffer_load_dwordx4 %0, %3, %4 offen lds\n\tbuffer_load_dwordx4 %1, %3, %4 offen offset:1024 lds\n\tbuffer_load_dwordx4 %2, %3, %4 offen offset:2048 lds"
               ::"v"(v0), "v"(v1), "v"(v2), "s"(srd), "s"(soff), "s"(m0v) : "memory");
}
DI void touch(i32x4 srd, unsigned voff, unsigned soff, unsigned m0v) {
  asm volatile("s_mov_b32 m0, %3\n\ts_nop 0\n\tbuffer_load_dword %0, %1, %2 offen lds" ::"v"(voff), "s"(srd), "s"(soff), "s"(m0v) : "memory");
}
typedef __amdgpu_buffer_rsrc_t rsrc_t;
DI rsrc_t mk(const void* p, unsigned bytes) { return __builtin_amdgcn_make_buffer_rsrc((void*)p, 0, bytes, 0x00020000); }
constexpr int SC1 = 16;
DI u32x4 gld128(rsrc_t r, unsigned v, unsigned s) { return __builtin_amdgcn_raw_buffer_load_b128(r, v, s, 0); }
DI u32x4 gld128c(rsrc_t r, unsigned v, unsigned s) { return __builtin_amdgcn_raw_buffer_load_b128(r, v, s, SC1); }
DI u32x2 gld64c(rsrc_t r, unsigned v, unsigned s) { return __builtin_amdgcn_raw_buffer_load_b64(r, v, s, SC1); }
DI void gst128c(rsrc_t r, unsigned v, u32x4 d) { __builtin_amdgcn_raw_buffer_store_b128(d, r, v, 0, SC1); }
DI void gst64c(rsrc_t r, unsigned v, unsigned s, u32x2 d) { __builtin_amdgcn_raw_buffer_store_b64(d, r, v, s, SC1); }

constexpr unsigned WS_EPOCH = 0;
constexpr unsigned WS_FLAG = 4096;
constexpr unsigned FL_G = 0;
constexpr unsigned FL_K = 128;
constexpr unsigned FL_A = 256;
constexpr unsigned FL_O = 384;
#define PRS 4480
#define YS 4480
#define KS 5248
#define OS 20864
constexpr unsigned WS_PART = 1u << 18;
constexpr unsigned WS_O = 1u << 20;
constexpr unsigned WS_KVT = 4u << 20;
constexpr unsigned WS_YT = 8u << 20;
constexpr unsigned WS_DBG = 12u << 20;
constexpr unsigned WS_BYTES = 16u << 20;

DI void set_flag(char* ws, unsigned idx, unsigned tag) { __hip_atomic_store((GAS unsigned*)(ws + WS_FLAG + idx * 128), tag, __ATOMIC_RELAXED, __HIP_MEMORY_SCOPE_AGENT); }
DI void wait_flags(char* ws, unsigned idx, unsigned tag) {
  GAS unsigned* p = (GAS unsigned*)(ws + WS_FLAG + idx * 128);
#pragma unroll 1
  for (int spins = 0; spins < (1 << 18); ++spins) {
    const unsigned v = __hip_atomic_load(p, __ATOMIC_RELAXED, __HIP_MEMORY_SCOPE_AGENT);
    if (__all(v == tag)) break;
    __builtin_amdgcn_s_sleep(1);
  }
  asm volatile("" ::: "memory");
}
DI unsigned ld_flag(char* ws, unsigned idx) { return __hip_atomic_load((GAS unsigned*)(ws + WS_FLAG + idx * 128), __ATOMIC_RELAXED, __HIP_MEMORY_SCOPE_AGENT); }
constexpr unsigned EMPTY = 0xffffffffu;
constexpr int SWEEPS = 1 << 16;
DI unsigned umax3(unsigned a, unsigned b, unsigned c) { return __builtin_elementwise_max(__builtin_elementwise_max(a, b), c); }
DI unsigned mx4(unsigned m, u32x4 v) { return umax3(umax3(m, v[0], v[1]), v[2], v[3]); }
DI bool none_empty(unsigned m) { return __builtin_amdgcn_ballot_w64(m == EMPTY) == 0; }
#define PIN(x) asm volatile("" : "+v"(x))
#define EV(k)
#define STINS "s_memrealtime"
#define FEN 1
#define KEEP 0
#define ST(n) do { if (STAMP == 2 && (n) != 0 && (n) != 7 && !(KEEP >> (n) & 1)) { if (FEN) { SCHED(); if (FEN == 2) asm volatile("s_waitcnt lgkmcnt(0)" ::: "memory"); SCHED(); } break; } SCHED(); u64 t_; asm volatile(STINS " %0\n\ts_waitcnt lgkmcnt(0)" : "=s"(t_) :: "memory"); asm volatile("v_writelane_b32 %0, %1, %2" : "+v"(stampv) : "s"((unsigned)t_), "i"(n)); SCHED(); } while (0)
#define STEND() do { if (blockIdx.x == 40 && wv == 4) { const unsigned d_ = __builtin_amdgcn_readlane(stampv, 14) - __builtin_amdgcn_readlane(stampv, 0); const u64 u_ = __builtin_amdgcn_s_memrealtime() + 20ull * (d_ < 10000u ? d_ : 10000u); while (__builtin_amdgcn_s_memrealtime() < u_) __builtin_amdgcn_s_sleep(4); } } while (0)

constexpr int SLOT = 40960, XB = 16384, WB = 12288;
constexpr int G_RED = 36864;
constexpr int L_DONE = 161792;
constexpr int L_CNT = 161808;
constexpr int L_PAIR = 161824;
constexpr int L_DUMMY = 163328;
constexpr int WOP[4] = {131072, 45056, 81920, 0};
constexpr int G_OUT = 112640;
constexpr int HCP = 4640, HCH = 4 * HCP;
constexpr int A_X = 4 * HCH, XCH = 4224;
constexpr int A_H4 = A_X + 5 * XCH;
constexpr int KP = 36 * 256, VP = 32 * 256;
constexpr int A_K = A_X, A_V = A_X + 3 * KP;
constexpr int A_DMP = 113920;
constexpr int T_SCR = 61440;

__global__ void __launch_bounds__(512, 2)
fused_kernel(const u16* __restrict__ Aq, const u16* __restrict__ Ak, const u16* __restrict__ Av, const u16* __restrict__ Ask,
             const u16* __restrict__ Ah, const u16* __restrict__ AD, const u16* __restrict__ Bx, const u16* __restrict__ Bw,
             const u16* __restrict__ Bo, u16* __restrict__ out, char* __restrict__ ws) {
  __shared__ __attribute__((aligned(16))) char smem[163840];
  const int b = blockIdx.x, xcd = b & 7, j = (b >> 3) & 15;
  int role = b >> 7;
  const int tid = threadIdx.x, lane = tid & 63;
  int wv = __builtin_amdgcn_readfirstlane(tid >> 6);
  const unsigned L = (unsigned)(size_t)(LAS char*)smem;
  unsigned stampv = 0; (void)stampv;
  ST(0);
  asm volatile("" ::"s"(Aq), "s"(Ak), "s"(Av), "s"(Ask), "s"(Ah), "s"(AD), "s"(Bx), "s"(Bw), "s"(Bo), "s"(out), "s"(ws));
  const int rb = (xcd >> 1) * 4 + (j >> 2), cg = (xcd & 1) * 4 + (j & 3);
  const int a0 = (160 * cg) & ~63, dl = 160 * cg - a0;
  const int hf = xcd & 1, ai = (xcd >> 1) * 16 + j;
  const i32x4 sx = make_srd(Bx, 2048 * 640 * 2), sw = make_srd(Bw, 640 * 1920 * 2);

  unsigned va[3], vb[3], vx = 0;
  int sp = 0;
  u32x2 wk;
  auto issue_x = [&](int slot) {
#pragma unroll
    for (int u = 0; u < 4; u++) dma16(sx, vx, sp * 128 + u * 32 * 1280, L + slot * SLOT + ((wv & 3) + 4 * u) * 1024);
  };
  auto issue_w = [&](int slot) {
    const int lw = wv & 3; const unsigned lb = L + slot * SLOT, so = (sp * 64 + 16 * lw) * 3840 + 1280 + a0;
    dma16x3(sw, va[0], va[1], va[2], so, lb + XB + lw * 3072);
    dma16x3(sw, vb[0], vb[1], vb[2], so + 1280, lb + XB + WB + lw * 3072);
    sp++;
  };
  auto set_warm = [&]() { const unsigned r = (lane * 26) >> 8, l = lane - r * 10;
    va[0] = (((xcd >> 1) * 512 + 32 * j + lane) * 1280) | (lane < 32 ? 0u : 0xf0000000u);
    va[1] = ((4 * j + r) * 3840 + (l >= 5 ? 640 : 0) + l * 128) | (lane < 40 ? 0u : 0xf0000000u); };
  auto warm = [&](int s) { touch(sx, va[0], s * 128, L + L_DUMMY); touch(sw, va[1], s * 64 * 3840 + 1280 + (xcd & 1) * 640, L + L_DUMMY); };
  const rsrc_t rws = mk(ws, WS_BYTES);
  unsigned tag = 0;
  if (wv == 7) {
    tag = *(const __attribute__((address_space(4))) unsigned*)(ws + WS_EPOCH) + 1u;
    if (role == 0) {
#pragma unroll
      for (int i = 0; i < 4; i++) gst128c(rws, WS_PART + (cg * 16 + rb) * PRS + lane * 16 + i * 1024, (u32x4){EMPTY, EMPTY, EMPTY, EMPTY});
    }
    asm volatile("" : "+s"(tag));
  }
  if (role == 0) {
    if (wv < 4) {
      { const int row = 8 * wv + (lane >> 3); vx = (rb * 128 + row) * 1280 + (((lane & 7) ^ ((row >> 1) & 7)) << 4); }
      ST(13);
      issue_x(0);
#pragma unroll
      for (int u = 0; u < 3; u++) { const unsigned c = u * 64 + lane, row = (c * 171) >> 11, pos = c - row * 12; va[u] = row * 3840 + ((pos ^ ((row >> 3) << 1)) << 4) - 1024 * u; vb[u] = va[u] ^ 16; }
      issue_w(0);
    } else if (wv == 6) {
      u64 pc; asm volatile("s_getpc_b64 %0" : "=s"(pc));
      touch(make_srd((const void*)(pc & ~127ull), CODEB), (lane * 16 + j) * 128, 0, L + L_DUMMY);
    }
  } else {
    const i32x4 sk_ = make_srd(Ak, 2048 * 1280), sv_ = make_srd(Av, 2048 * 1280);
    const unsigned rl = lane >> 4, base = (32 * ai + 4 * wv + rl) * 1280 + hf * 640;
    const unsigned ck = ((lane ^ rl ^ (((wv + 1) & 6) << 1)) & 15) << 4, cv = ((lane ^ rl ^ ((wv & 6) << 1)) & 15) << 4;
    if (wv + P0 <= P1) { set_warm(); warm(wv + P0); }
    asm volatile("global_load_dwordx2 %0, %1, %2" : "=v"(wk) : "v"((320 * hf + 16 * wv + (rl < 2 || (rl == 2 && wv < 4) ? 128 * rl : 0) + (lane & 15)) * 8), "s"(Ask) : "memory");
#pragma unroll
    for (int u = 0; u < 3; u++) {
      dma16(sk_, (base + ck) | (u == 2 ? (ck & 128) << 24 : 0), u * 256, L + A_K + u * KP + (wv + 1) * 1024);
      dma16(sv_, (base + cv) | (u == 2 ? (cv & 128) << 24 : 0), u * 256, L + A_V + u * VP + wv * 1024); }
    if (wv < 3) { const unsigned c0 = ((lane ^ rl) & 15) << 4;
      dma16(sk_, ((32 * ai - 4 + rl) * 1280 + hf * 640 + c0 + wv * 256) | (wv == 2 ? (c0 & 128) << 24 : 0), 0, L + A_K + wv * KP); }
    ST(13);
  }
  asm volatile("" : "+s"(role), "+s"(wv));
  const int lw = wv & 3, g = lane >> 4, r16 = lane & 15, q4 = r16 >> 2, p4 = lane & 3;
  const rsrc_t ro = mk(out, 2048 * 1280 * 2);
  auto wait_cnt = [&](unsigned need = 4) {
#pragma unroll 1
    for (int it = 0; it < (1 << 20) && __builtin_amdgcn_readfirstlane(*(volatile LAS unsigned*)(size_t)(L + L_CNT)) < need; it++) __builtin_amdgcn_s_sleep(1);
    asm volatile("" ::: "memory"); };
  auto arrive = [&]() { if (lane == 0) __hip_atomic_fetch_add((LAS unsigned*)(size_t)(L + L_CNT), 1u, __ATOMIC_RELAXED, __HIP_MEMORY_SCOPE_WORKGROUP); };
  const u32x4 emp = {EMPTY, EMPTY, EMPTY, EMPTY};

  if (role == 0) {
    const int cg3 = cg, rt0 = rb * 8, a3 = a0, dl3 = dl;
    if (wv < 4) {
      auto issue_q = [&](int pos) {
        const unsigned so = ((sp - 10) * 64 + 16 * lw) * 3840 + a0;
        dma16x3(sw, va[0], va[1], va[2], so, L + pos + lw * 3072);
        sp++;
      };
      sp = 1;
      PIN(vb[0]); PIN(vb[1]); PIN(vb[2]);
      issue_x(1); issue_w(1);
      ST(15);
      unsigned aX[2]; { const int row = lw * 32 + r16;
#pragma unroll
        for (int kk = 0; kk < 2; kk++) aX[kk] = L + row * 128 + (((4 * kk + g) ^ ((row >> 1) & 7)) << 4); }
      u32x4 xq[10][2][2];
      constexpr int QP[10] = {45056, 45056 + WB, 45056 + 2 * WB, 81920, 81920 + WB, 81920 + 2 * WB, 81920 + 3 * WB, 0, WB, 2 * WB};
      unsigned aQ[5];
#pragma unroll
      for (int n = 0; n < 5; n++) { const int off = (n < 4 ? dl + 40 * n + 8 * p4 : dl + 40 * p4 + 32) ^ ((g & 1) << 5); aQ[n] = L + QP[0] + (8 * g + q4) * 192 + off; PIN(aQ[n]); }
      unsigned aE[6]; { const int col = p4 == 0 ? 8 + q4 : 0; aE[0] = L + G_RED + lane * 16; aE[1] = L + G_RED + r16 * 16; aE[2] = L + G_RED + (16 * g + col) * 16; aE[3] = L + G_RED + col * 16;
        aE[4] = aE[2] ^ 32; aE[5] = aE[3] ^ 32; }
      const unsigned ob = WS_O + (rb * 8 + lw * 2) * OS + (cg >> 1) * 5120;
      unsigned o16[2] = {ob + (cg & 1) * 2048 + lane * 16, ob + (cg & 1) * 2048 + lane * 16 + OS}, o8 = ob + 4096 + (cg & 1) * OS + lane * 16;
      PIN(aE[0]); PIN(aE[1]); PIN(aE[2]); PIN(aE[3]); PIN(aE[4]); PIN(aE[5]); PIN(o16[0]); PIN(o16[1]); PIN(o8);
      unsigned aP = L + (lane ? L_DUMMY + lane * 4 : L_DONE); PIN(aP);
#pragma unroll
      for (int s = 0; s < 10; s++) {
        if (s < 9) WAITV(10); else WAITV(9);
        WAITL0(); BAR();
        if (s < 8) issue_x((s + 2) % 3); else issue_q(QP[s == 8 ? 0 : 3]);
#pragma unroll
        for (int kk = 0; kk < 2; kk++)
#pragma unroll
          for (int m = 0; m < 2; m++) xq[s][kk][m] = ld128(aX[kk] + (s % 3) * SLOT, m * 2048);
        if (s < 8) issue_w((s + 2) % 3);
        else if (s == 8) { issue_q(QP[1]); issue_q(QP[2]); }
        else { issue_q(QP[4]); issue_q(QP[5]); }
      }
      WAITL0(); BAR();
      issue_q(QP[6]); issue_q(QP[7]); issue_q(QP[8]); issue_q(QP[9]);
      f32x4 qa[2][5];
      ST(1);
      {
        u32x2 fw[2][5][2];
        auto rdw = [&](int ks, int n, int h) {
          if (h == 0 && ks == 6) { aQ[n] += QP[3] - QP[0]; asm volatile("" : "+v"(aQ[n])); }
          if (h == 0 && ks == 14) { aQ[n] -= QP[3]; asm volatile("" : "+v"(aQ[n])); }
          fw[ks & 1][n][h] = ldtr(aQ[n], (ks - (ks < 6 ? 0 : ks < 14 ? 6 : 14)) * 32 * 192 + 768 * h); };
        ST(8); WAITV(0); BAR(); ST(10);
#pragma unroll
        for (int n = 0; n < 5; n++) { rdw(0, n, 0); rdw(0, n, 1); }
        SCHED();
#pragma unroll
        for (int ks = 0; ks < 20; ks++) {
          if (ks == 5 || ks == 13 || ks == 19) { asm volatile("ds_add_u32 %0, %1" ::"v"(aP), "v"(1u << (ks == 5 ? 0 : ks == 13 ? 8 : 16)) : "memory"); SCHED(); }
#pragma unroll
          for (int n = 0; n < 5; n++) {
            if (n == 0) WAITL(4); else if (n == 3) WAITL(6);
            SCHED();
            const u32x4 w = cat(fw[ks & 1][n][0], fw[ks & 1][n][1]);
            if (ks) MFMA_ACC(qa[0][n], w, xq[ks >> 1][ks & 1][0]); else MFMA_ONE(qa[0][n], w, xq[0][0][0]);
            SCHED();
            if (ks < 19) rdw(ks + 1, n, 0);
            SCHED();
            if (ks) MFMA_ACC(qa[1][n], w, xq[ks >> 1][ks & 1][1]); else MFMA_ONE(qa[1][n], w, xq[0][0][1]);
            SCHED();
            if (ks < 19) rdw(ks + 1, n, 1);
            SCHED();
          }
        }
        asm volatile("s_nop 7" : "+v"(qa[0][4]), "+v"(qa[1][4]), "+v"(qa[0][3]), "+v"(qa[1][3]), "+v"(qa[0][2]), "+v"(qa[1][2]), "+v"(qa[0][1]), "+v"(qa[1][1]), "+v"(qa[0][0]), "+v"(qa[1][0]));
      }
      ST(11);
      u32x4 bq[2][4];
      u32x2 e0[4][2], e1[4][2];
      f32x4 n0[2][4], n1[2][4]; float rc[2][4];
      auto omul = [&](int m, int i) { const int h = i & 3;
        if (i < 4) n1[m][h] = mfma16(cat(e1[h][0], e1[h][1]), bq[m][h], (f32x4){0, 0, 0, 0}); else n0[m][h] = mfma16(cat(e0[h][0], e0[h][1]), bq[m][h], (f32x4){0, 0, 0, 0});
        SCHED(); };
      auto get_state = [&]() {
        asm volatile("ds_add_u32 %0, %1" ::"v"(aP), "v"(1u << 24) : "memory");
        ST(2); BAR(); ST(3);
#pragma unroll
        for (int h = 0; h < 4; h++) { e1[h][0] = ld64(aE[h < 2 ? 2 : 4], (h >> 1) * 2048 + (h & 1) * 64); SCHED(); e1[h][1] = ld64(aE[h < 2 ? 3 : 5], (h >> 1) * 2048 + (h & 1) * 320 + 520); SCHED(); }
#pragma unroll
        for (int h = 0; h < 4; h++) { e0[h][0] = ld64(aE[0], 4096 + (h >> 1) * 2048 + 8 * (h & 1)); SCHED(); e0[h][1] = ld64(aE[1], (h >> 1) * 2048 + (h & 1) * 256 + 8); SCHED(); } };
#pragma unroll
      for (int m = 0; m < 2; m++) {
        f32x2 u[10], a[10];
#pragma unroll
        for (int i = 0; i < 10; i++) { u[i] = (f32x2){qa[m][i >> 1][2 * (i & 1)], qa[m][i >> 1][2 * (i & 1) + 1]}; a[i] = u[i] * 1.44269504088896341f - 1.44269504088896341f; }
        SCHED();
#pragma unroll
        for (int i = 0; i < 10; i++) { a[i] = (f32x2){__builtin_amdgcn_exp2f(a[i][0]), __builtin_amdgcn_exp2f(a[i][1])}; SCHED(); if (EARLY && m && i >= 2) omul(0, i - 2); }
#pragma unroll
        for (int i = 0; i < 10; i++) a[i] = (f32x2){__builtin_amdgcn_fmed3f(u[i][0], a[i][0], 1.f), __builtin_amdgcn_fmed3f(u[i][1], a[i][1], 1.f)};
        SCHED();
#pragma unroll
        for (int i = 0; i < 10; i++) a[i] += 1e-6f;
        const unsigned t0 = pk(a[8][0], a[8][1]), t1 = pk(a[9][0], a[9][1]);
#pragma unroll
        for (int h = 0; h < 4; h++) { bq[m][h] = (u32x4){pk(a[2 * h][0], a[2 * h][1]), pk(a[2 * h + 1][0], a[2 * h + 1][1]), g == h ? t0 : 0u, g == h ? t1 : 0u}; PIN(bq[m][h]); }
        SCHED();
        if (m == (EARLY ? 0 : 1)) get_state();
      }
      if (!EARLY)
#pragma unroll
        for (int i = 0; i < 8; i++) omul(0, i);
      const unsigned pw = *(volatile LAS unsigned*)(size_t)aP;
      SCHED();
#pragma unroll
      for (int i = 0; i < 8; i++) { omul(1, i); if (i & 1) rc[0][i >> 1] = __builtin_amdgcn_rcpf(rc[0][i >> 1]); else rc[0][i >> 1] = add1(n1[0][i >> 1][1], 1e-6f); SCHED(); }
      if (!(__builtin_amdgcn_readfirstlane(pw) & 0x8000)) {
#pragma unroll 1
        for (int it = 0; it < (1 << 22) && !(__builtin_amdgcn_readfirstlane(*(volatile LAS unsigned*)(size_t)aP) & 0x8000); it++) __builtin_amdgcn_s_sleep(1);
        asm volatile("" ::: "memory"); }
#pragma unroll
      for (int m = 0; m < 2; m++) {
        if (m)
#pragma unroll
          for (int h = 0; h < 4; h++) rc[1][h] = __builtin_amdgcn_rcpf(n1[1][h][1] + 1e-6f);
#pragma unroll
        for (int hp_ = 0; hp_ < 2; hp_++) { const f32x4 a = n0[m][2 * hp_] * rc[m][2 * hp_], c2 = n0[m][2 * hp_ + 1] * rc[m][2 * hp_ + 1];
          gst128c(rws, o16[m] + hp_ * 1024, (u32x4){pk(a[0], a[1]), pk(a[2], a[3]), pk(c2[0], c2[1]), pk(c2[2], c2[3])}); SCHED(); }
      }
      gst128c(rws, o8, (u32x4){pk(mul1(n1[0][0][0], rc[0][0]), mul1(n1[0][1][0], rc[0][1])), pk(mul1(n1[0][2][0], rc[0][2]), mul1(n1[0][3][0], rc[0][3])),
                               pk(mul1(n1[1][0][0], rc[1][0]), mul1(n1[1][1][0], rc[1][1])), pk(mul1(n1[1][2][0], rc[1][2]), mul1(n1[1][3][0], rc[1][3]))});
      asm volatile("ds_add_u32 %0, %1 offset:16" ::"v"(aP), "v"(1u) : "memory");
      ST(12);
    } else {
      const int rg = lw >> 1, c = lw & 1;
      unsigned aA[2]; { const int row = rg * 64 + r16;
#pragma unroll
        for (int kk = 0; kk < 2; kk++) aA[kk] = L + row * 128 + (((4 * kk + g) ^ ((row >> 1) & 7)) << 4); }
      unsigned aB[5];
#pragma unroll
      for (int n = 0; n < 5; n++) { int sec, off; if (n < 4) { sec = n >> 1; off = dl + 80 * c + 40 * (n & 1) + 8 * p4; } else { sec = p4 >> 1; off = dl + 80 * c + 40 * (p4 & 1) + 32; }
        off ^= ((g & 1) << 5) ^ (sec << 4); aB[n] = L + XB + sec * WB + (8 * g + q4) * 192 + off; }
      if (lane < 12) *(LAS unsigned*)(size_t)(L + L_DONE + lane * 4) = 0;
      f32x4 acc[4][5];
      u32x4 fa[2][4]; u32x2 fb[2][5][2];
      const float i4 = r16 < 8 ? 1.f : 0.f;
      f32x4 c4 = {i4, i4, i4, i4}; PIN(c4);
      unsigned aA2[2], aB2[5];
#pragma unroll
      for (int i = 0; i < 2; i++) { aA2[i] = aA[i] + 2 * SLOT; PIN(aA[i]); PIN(aA2[i]); }
#pragma unroll
      for (int i = 0; i < 5; i++) { aB2[i] = aB[i] + 2 * SLOT; PIN(aB[i]); PIN(aB2[i]); }
      auto rd = [&](int t, int slot, int kk) {
        const int o = slot == 1 ? SLOT : 0;
        if (t < 4) fa[kk][t] = ld128(slot == 2 ? aA2[kk] : aA[kk], o + t * 2048);
        else { const int n2 = (t - 4) >> 1, h = (t - 4) & 1; fb[kk][n2][h] = ldtr(slot == 2 ? aB2[n2] : aB[n2], o + kk * 32 * 192 + h * 768); } };
      auto half = [&](int slot, int kk, bool first = false) {
#pragma unroll
        for (int t = 0; t < 20; t++) {
          const u32x4 w = cat(fb[1 - kk][t >> 2][0], fb[1 - kk][t >> 2][1]);
          if (!first) MFMA_ACC(acc[t & 3][t >> 2], fa[1 - kk][t & 3], w);
          else if (t < 8) MFMA_ONE(acc[t & 3][t >> 2], fa[1 - kk][t & 3], w);
          else if (t < 16) MFMA_NEW(acc[t & 3][t >> 2], fa[1 - kk][t & 3], w);
          else MFMA_INI(acc[t & 3][t >> 2], fa[1 - kk][t & 3], w, c4);
          SCHED();
          if (t < 14) rd(t, slot, kk);
          if (t == 18) WAITL0();
          SCHED();
        }
      };
      unsigned vo[3], aW[3][3][2], go[5], pvo = g * PRS + r16 * 16, aS = L + G_RED + (lw ^ 2) * 2048 + ((g >> 1) * 16 + r16) * 16 + (g & 1) * 8;
      u32x4 sel[2];
#pragma unroll
      for (int u = 0; u < 3; u++) { const unsigned cc = u * 64 + lane, row = (cc * 171) >> 11, ps = cc - row * 12; vo[u] = row * 1280 + ((ps ^ (((row >> 2) & 1) << 1)) << 4) - 1024 * u; PIN(vo[u]); }
#pragma unroll
      for (int o = 0; o < 2; o++) {
#pragma unroll
        for (int i = 0; i < 4; i++) sel[o][i] = ((r16 >> 1) == 4 * o + i) ? 0x3f80u << ((r16 & 1) * 16) : 0u;
        PIN(sel[o]); }
#pragma unroll
      for (int e = 0; e < 2; e++)
#pragma unroll
        for (int r = 0; r < 3; r++) {
          aW[2][r][e] = L + WOP[3] + (r < 2 ? 4 * g + q4 : 20 * q4 + 16 + (g ^ (q4 & 2))) * 192 + ((dl3 + 32 * e + 8 * p4) ^ (((r < 2 ? g + r : q4) & 1) << 5));
          aW[0][r][e] = aW[2][r][e] + WOP[0] - WOP[3]; aW[1][r][e] = aW[2][r][e] + WOP[1] - WOP[3]; PIN(aW[0][r][e]); PIN(aW[1][r][e]); PIN(aW[2][r][e]); }
#pragma unroll
      for (int u = 0; u < 5; u++) { const unsigned c = lane + 64 * u, row = (c * 6554) >> 16; go[u] = row * 2400 + c * 16 + (rt0 + 2 * lw) * 16 * 2560 + 1280 + cg3 * 160; PIN(go[u]); }
      PIN(pvo); PIN(aS);
      const int h1 = xcd & 1;
      unsigned fv = 0;
      __builtin_amdgcn_s_setprio(2);
      WAITL0(); BAR(); ST(14);
#pragma unroll
      for (int t = 0; t < 14; t++) { rd(t, 0, 0); SCHED(); }
      WAITL0(); SCHED();
      half(0, 1, true);
#pragma unroll 1
      for (int s = 0; s < 3; s++) {
        BAR(); SCHED(); half(1, 0); half(1, 1);
        BAR(); SCHED(); half(2, 0); half(2, 1);
        BAR(); if (s == 0) ST(15); SCHED(); half(0, 0); half(0, 1);
        if (s == 0 && wv == 7) { WAITV(0); if (lane == 0) set_flag(ws, FL_G + rb * 8 + cg, tag); }
      }
      BAR(); SCHED();
      const int irow = c * 2 + rg;
      ST(1);
      u32x4 T[2][6]; f32x4 R[7];
#pragma unroll
      for (int t = 0; t < 20; t++) { const int nn = t >> 2, n = nn < 3 ? nn + 2 : nn - 3, m = t & 3;
        const u32x4 w = cat(fb[1][n][0], fb[1][n][1]);
        MFMA_ACC(acc[m][n], fa[1][m], w); SCHED();
        if (t >= 8) {
#pragma unroll
          for (int z = 0; z < 2; z++) { const int i = 2 * (t - 8) + z, tn = i >> 3, tm = (i >> 1) & 3;
            const unsigned v = pk(acc[tm][tn + 2][2 * z], acc[tm][tn + 2][2 * z + 1]);
            T[tm >> 1][tn < 2 ? tn + 2 : 5][2 * (tm & 1) + z] = v; }
          SCHED(); } }
#pragma unroll
      for (int i = 0; i < 8; i++) T[i >> 2][5][i & 3] = r16 < 8 ? 0x3f803f80u : T[i >> 2][5][i & 3];
      SCHED();
      auto rmul = [&](int ks, int i) { constexpr int ta[7] = {0, 1, 0, 1, 4, 4, 4}, tb[7] = {2, 3, 5, 5, 2, 3, 5};
        R[i] = mfma16(T[ks][ta[i]], T[ks][tb[i]], ks ? R[i] : (f32x4){0, 0, 0, 0}); };
#pragma unroll
      for (int ks = 0; ks < 2; ks++) {
        f32x2 u[12], a[12];
#pragma unroll
        for (int i = 0; i < 12; i++) { const int n = i < 4 ? 4 : (i >> 2) - 1, m = 2 * ks + ((i >> 1) & 1);
          u[i] = (f32x2){acc[m][n][2 * (i & 1)], acc[m][n][2 * (i & 1) + 1]}; a[i] = u[i] * 1.44269504088896341f - 1.44269504088896341f; }
        SCHED();
#pragma unroll
        for (int i = 0; i < 12; i++) { if (ks && i < 7) { rmul(0, i); SCHED(); }
          a[i] = (f32x2){__builtin_amdgcn_exp2f(a[i][0]), __builtin_amdgcn_exp2f(a[i][1])}; SCHED(); }
#pragma unroll
        for (int i = 0; i < 12; i++) a[i] = (f32x2){__builtin_amdgcn_fmed3f(u[i][0], a[i][0], 1.f), __builtin_amdgcn_fmed3f(u[i][1], a[i][1], 1.f)};
        SCHED();
#pragma unroll
        for (int i = 0; i < 12; i++) a[i] += 1e-6f;
        SCHED();
        if (ks == 0 && wv == 7) fv = ld_flag(ws, FL_G + r16 * 8 + cg);
        if (ks == 1 && B11) { ST(8); BAR(); ST(9); }
        SCHED();
#pragma unroll
        for (int i = 0; i < 12; i++) { T[ks][i < 4 ? 4 : (i >> 2) - 1][i & 3] = pk(a[i][0], a[i][1]); SCHED();
          if (ks && i >= 5 && i < 8) { rmul(1, i - 1); SCHED(); } if (ks && (i == 9 || i == 10)) { rmul(1, 2 * (10 - i)); SCHED(); } }
      }
      rmul(1, 3); rmul(1, 1);
      SCHED();
      f32x4 M23, M456;
#pragma unroll
      for (int i = 0; i < 4; i++) {
        M23[i] = (q4 & 1) ? R[3][i] : R[2][i];
        const float r45 = (g == 0) ? R[4][i] : R[5][i];
        M456[i] = __uint_as_float(__builtin_amdgcn_permlane32_swap(__float_as_uint(r45), __float_as_uint(R[6][i]), false, false)[0]);
      }
      f32x4 k0, k1;
      const unsigned aR = L + G_RED + lw * 2048 + lane * 16, aR2 = L + G_RED + (lw ^ 2) * 2048 + lane * 16;
      if (rg) { k0 = M23; k1 = M456; st128(aR, 0, __builtin_bit_cast(u32x4, R[0])); st128(aR, 1024, __builtin_bit_cast(u32x4, R[1])); }
      else { k0 = R[0]; k1 = R[1]; st128(aR, 0, __builtin_bit_cast(u32x4, M23)); st128(aR, 1024, __builtin_bit_cast(u32x4, M456)); }
      if (B11) {
        asm volatile("" ::: "memory");
        *(volatile LAS unsigned*)(size_t)(L + L_PAIR + lw * 4) = 1u;
#pragma unroll 1
        for (int it = 0; it < (1 << 22) && !__builtin_amdgcn_readfirstlane(*(volatile LAS unsigned*)(size_t)(L + L_PAIR + (lw ^ 2) * 4)); it++) {}
        asm volatile("" ::: "memory");
      } else { ST(8); WAITL0(); BAR(); ST(9); }
      const bool pset = __all(fv == tag);
      { const f32x4 o0 = __builtin_bit_cast(f32x4, ld128(aR2, 0)), o1 = __builtin_bit_cast(f32x4, ld128(aR2, 1024));
#pragma unroll
        for (int i = 0; i < 4; i++) { k0[i] = add1(k0[i], o0[i]); k1[i] = add1(k1[i], o1[i]); } }
      gst128c(rws, lane * 16 + WS_PART + (cg * 16 + rb) * PRS + irow * 1024, (u32x4){pk(k0[0], k0[1]), pk(k0[2], k0[3]), pk(k1[0], k1[1]), pk(k1[2], k1[3])});
      ST(2);
      if (wv == 7) { if (!pset) { EV(7); wait_flags(ws, FL_G + r16 * 8 + cg, tag); }
        if (lane == 0) __hip_atomic_fetch_add((LAS unsigned*)(size_t)(L + L_DONE), 0x80u, __ATOMIC_RELAXED, __HIP_MEMORY_SCOPE_WORKGROUP); }
      if (CPRIO == 0) __builtin_amdgcn_s_setprio(0);
      ST(10);
      u32x4 pv[4][4];
      const i32x4 so_ = make_srd(Bo, 640 * 640 * 2);
      int gi = lw;
      const unsigned fo = FL_O + rt0 + (lane & 7);
      auto prog = [&]() { return (unsigned)__builtin_amdgcn_readfirstlane(*(volatile LAS unsigned*)(size_t)(L + L_DONE)); };
      auto level = [&]() { return __builtin_popcount(prog() & 0x04040404u); };
      auto wo_groups = [&](int n, int lv) {
#pragma unroll 1
        for (; n > 0 && gi < 40 && gi < 10 * lv + 10; n--, gi += 4) { const int pos = (gi < 10 ? WOP[0] : gi < 20 ? WOP[1] - 30720 : gi < 30 ? WOP[2] - 61440 : WOP[3] - 92160) + gi * 3072;
          dma16x3(so_, vo[0], vo[1], vo[2], (gi + (h1 ? (gi < 20 ? 20 : -20) : 0)) * 16 * 1280 + a3, L + pos); } };
      wo_groups(10, 0);
      const unsigned pb = WS_PART + cg * 16 * PRS + irow * 1024, gr = (rb >> 2) ^ 3;
      const i32x4 sws = make_srd(ws, WS_BYTES);
#pragma unroll 1
      for (int it = 0; it < (1 << 20) && (prog() & (PRE ? 0x84u : 0x80u)) != (PRE ? 0x84u : 0x80u); it++) __builtin_amdgcn_s_sleep(1);
      asm volatile("" ::: "memory");
      if (PRE == 1) wo_groups(10, 1);
      if (GD) __builtin_amdgcn_s_sleep(GD);
      ST(13);
      f32x4 sum[2];
      auto reduce = [&]() {
#pragma unroll
        for (int i = 0; i < 8; i++)
#pragma unroll
          for (int bp = 0; bp < 2; bp++) { sum[bp] = mfma16(sel[i & 1], pv[i >> 1][2 * bp + (i & 1)], i ? sum[bp] : (f32x4){0, 0, 0, 0}); SCHED(); } };
#pragma unroll
      for (int i = 0; i < 16; i++) { pv[i >> 2][i & 3] = gld128c(rws, pvo + (i & 3) * 256, pb + (gr ^ (i >> 2)) * 4 * PRS); SCHED(); }
      if (wv == 7) fv = ld_flag(ws, fo);
      SCHED();
      reduce();
      const bool oset = __all(fv == tag);
      SCHED();
      wo_groups(PG, level());
      if (__builtin_amdgcn_ballot_w64(__builtin_isunordered(sum[0][0], sum[1][0]))) {
      EV(0);
      unsigned pend = 15;
#pragma unroll 1
      for (int it = 0; it < SWEEPS; it++) {
#pragma unroll
        for (int rp = 0; rp < 4; rp++) if ((pend >> rp & 1) && none_empty(mx4(mx4(mx4(mx4(0, pv[rp][0]), pv[rp][1]), pv[rp][2]), pv[rp][3]))) pend &= ~(1u << rp);
        if (!pend) break;
        EV(1);
#define GRP(r, s, a, b, c, d) "s_bitcmp1_b32 %18, " #r "\n\ts_cbranch_scc0 " #r "f\n\tbuffer_load_dwordx4 %" #a ", %16, %17, %" #s " offen sc1\n\tbuffer_load_dwordx4 %" #b ", %16, %17, %" #s " offen offset:256 sc1\n\t" \
                              "buffer_load_dwordx4 %" #c ", %16, %17, %" #s " offen offset:512 sc1\n\tbuffer_load_dwordx4 %" #d ", %16, %17, %" #s " offen offset:768 sc1\n" #r ":\n\t"
        asm volatile("s_nop 4\n\t" GRP(0, 19, 0, 1, 2, 3) GRP(1, 20, 4, 5, 6, 7) GRP(2, 21, 8, 9, 10, 11) GRP(3, 22, 12, 13, 14, 15) "s_waitcnt vmcnt(0)"
                     : "+v"(pv[0][0]), "+v"(pv[0][1]), "+v"(pv[0][2]), "+v"(pv[0][3]), "+v"(pv[1][0]), "+v"(pv[1][1]), "+v"(pv[1][2]), "+v"(pv[1][3]),
                       "+v"(pv[2][0]), "+v"(pv[2][1]), "+v"(pv[2][2]), "+v"(pv[2][3]), "+v"(pv[3][0]), "+v"(pv[3][1]), "+v"(pv[3][2]), "+v"(pv[3][3])
                     : "v"(pvo), "s"(sws), "s"(pend), "s"(pb + gr * 4 * PRS), "s"(pb + (gr ^ 1) * 4 * PRS), "s"(pb + (gr ^ 2) * 4 * PRS), "s"(pb + (gr ^ 3) * 4 * PRS) : "scc", "memory");
#undef GRP
      }
      reduce();
      }
      ST(3);
      {
#pragma unroll
        for (int bp = 0; bp < 2; bp++) st64(aS, bp * 512, (u32x2){pk(sum[bp][0], sum[bp][1]), pk(sum[bp][2], sum[bp][3])}); }
      auto o_ok = [&]() { if (lane == 0) __hip_atomic_fetch_add((LAS unsigned*)(size_t)(L + L_DONE), 0x8000u, __ATOMIC_RELAXED, __HIP_MEMORY_SCOPE_WORKGROUP); };
      if (wv == 7 && oset) o_ok();
      ST(11);
#pragma unroll 1
      for (int it = 0; it < (1 << 20) && gi < 40; it++) { const int lv = level(); if (lv == 4) break; if (gi < 10 * lv + 10) wo_groups(1, lv); else __builtin_amdgcn_s_sleep(2); }
      WAITL0(); BAR();
      ST(4);
      if (!oset) { EV(2); wait_flags(ws, fo, tag); o_ok(); }
      __builtin_amdgcn_s_setprio(0);
      wo_groups(10, 3);
      ST(12);
      const unsigned so = WS_O + (rt0 + 2 * lw) * OS, sc = L + (lw < 3 ? G_OUT + lw * 5120 : 30720), sp_[2] = {so + h1 * 10240, so + (h1 ^ 1) * 10240};
      unsigned lo[2] = {lane * 16u, lane * 16u + OS}, sw_ = sc + r16 * 160 + g * 8, sr = sc + lane * 16, lko = (((lane >> 1) & 3) ^ (2 * h1)) * 5120 + (lane & 1) * OS;
      PIN(sw_); PIN(sr); PIN(lo[0]); PIN(lo[1]); PIN(lko);
      WAITV(0); arrive(); wait_cnt(8);
      if (SD) __builtin_amdgcn_s_sleep(SD);
      f32x4 oa[2][5];
#pragma unroll 1
      for (int pass = 0; pass < 2; pass++) {
        u32x2 wf[3][5][2]; u32x4 of[6][2];
        auto look = [&]() { asm volatile("" ::: "memory"); return __builtin_amdgcn_raw_buffer_load_b32(rws, lko, so + 4096 + 1020, SC1); };
        auto await = [&](unsigned mask) {
#pragma unroll 1
          for (int it = 0; it < SWEEPS && (__builtin_amdgcn_ballot_w64(look() == EMPTY) & mask); it++) {}
          asm volatile("" ::: "memory"); };
        if (pass) { EV(5);
#pragma unroll 1
          for (int it = 0; it < SWEEPS; it++) { unsigned m = 0; asm volatile("" ::: "memory");
#pragma unroll 4
            for (int i = 0; i < 20; i++) m = mx4(mx4(m, gld128c(rws, lo[0], so + i * 1024)), gld128c(rws, lo[1], so + i * 1024));
            if (none_empty(m)) break; }
        }
        asm volatile("" ::: "memory");
        ST(5);
        auto rdo = [&](int ks, int n, int h) { const int p = ks / 5, f = ks % 5;
          wf[ks % 3][n][h] = ldtr(aW[(p + 1) >> 1][f < 4 ? h : 2][n & 1], (p == 2 ? WOP[2] - WOP[1] : 0) + (f < 4 ? f * 40 + 20 * h : 80 * h) * 192 + (n >> 1) * 64); };
        auto ldo = [&](int ks, int m) { of[ks % 6][m] = gld128c(rws, lo[m] + (ks % 10 & 3) * 1024, sp_[ks / 10] + (ks % 10 >> 2) * 4096); };
        unsigned lk;
#pragma unroll 1
        for (int it = 0; it < SWEEPS; it++) {
          lk = look();
          SCHED();
#pragma unroll
          for (int ks = 0; ks < 5; ks++) { ldo(ks, 0); SCHED(); ldo(ks, 1); SCHED(); }
#pragma unroll
          for (int i = 0; i < 10; i++) { rdo(i / 5, i % 5, 0); rdo(i / 5, i % 5, 1); }
          SCHED();
          if (!(__builtin_amdgcn_ballot_w64(lk == EMPTY) & 0xf)) break;
          EV(3);
          if (it >= RETRY) await(0xf);
        }
        SCHED();
#pragma unroll
        for (int ks = 0; ks < 20; ks++) {
          if (ks == 1) lk = look();
          if (ks == 5 && (__builtin_amdgcn_ballot_w64(lk == EMPTY) & 0xf0)) { EV(4); await(0xf0); }
#pragma unroll
          for (int n = 0; n < 5; n++) {
            if (n == 0) {
              if (ks >= 1 && ks < 5) __builtin_amdgcn_s_waitcnt(0x0a79); else if (ks < 16) __builtin_amdgcn_s_waitcnt(0x0a78); else if (ks == 16) __builtin_amdgcn_s_waitcnt(0x0a76);
              else if (ks == 17) __builtin_amdgcn_s_waitcnt(0x0a74); else if (ks == 18) __builtin_amdgcn_s_waitcnt(0x0a72); else __builtin_amdgcn_s_waitcnt(0x0070); }
            if (ks % 5 == 4 && n == 0) {
              const u32x4 e = of[ks % 6][0], o = of[ks % 6][1];
              of[ks % 6][0] = (u32x4){e[0], e[1], o[0], o[1]}; of[ks % 6][1] = (u32x4){e[2], e[3], o[2], o[3]};
              asm volatile("s_nop 1" : "+v"(of[ks % 6][0]), "+v"(of[ks % 6][1])); }
            if (ks < 15 && (n == 1 || n == 4)) ldo(ks + 5, n >> 2);
            SCHED();
            const u32x4 w = cat(wf[ks % 3][n][0], wf[ks % 3][n][1]);
            if (ks == 0) MFMA_NEW(oa[0][n], w, of[ks % 6][0]); else MFMA_ACC(oa[0][n], w, of[ks % 6][0]);
            SCHED();
            if (ks < 18) rdo(ks + 2, n, 0);
            SCHED();
            if (ks == 0) MFMA_NEW(oa[1][n], w, of[ks % 6][1]); else MFMA_ACC(oa[1][n], w, of[ks % 6][1]);
            SCHED();
            if (ks < 18) rdo(ks + 2, n, 1);
            SCHED();
          }
        }
        asm volatile("s_nop 7" : "+v"(oa[0][0]), "+v"(oa[0][1]), "+v"(oa[0][2]), "+v"(oa[0][3]), "+v"(oa[0][4]), "+v"(oa[1][0]), "+v"(oa[1][1]), "+v"(oa[1][2]), "+v"(oa[1][3]), "+v"(oa[1][4]));
        if (__builtin_amdgcn_ballot_w64(__builtin_isunordered(oa[0][0][0], oa[1][0][0])) == 0) break;
      }
      ST(6);
#pragma unroll
      for (int m = 0; m < 2; m++)
#pragma unroll
        for (int n = 0; n < 5; n++) { st64(sw_, m * 2560 + n * 32, (u32x2){pk(oa[m][n][0], oa[m][n][1]), pk(oa[m][n][2], oa[m][n][3])}); SCHED(); }
      { u32x4 r[5];
#pragma unroll
        for (int u = 0; u < 5; u++) r[u] = ld128(sr, u * 1024);
#pragma unroll
        for (int u = 0; u < 5; u++) __builtin_amdgcn_raw_buffer_store_b128(r[u], ro, go[u], 0, OUTB); }
    }
  } else {
    const int ch0 = 320 * hf + 5 * ai;
    const rsrc_t rh = mk(Ah, 640 * 4096);
    u32x4 idl, idh, dm, bw[3][2];
    { const unsigned hm = (g & 1) == (r16 >> 3) ? 0xffffu << ((r16 & 1) * 16) : 0u, one = g < 2 ? 0x3f803f80u : 0u;
#pragma unroll
      for (int i = 0; i < 4; i++) { dm[i] = ((r16 >> 1) & 3) == i ? hm : 0u; idl[i] = dm[i] & one; idh[i] = dm[i] & (one ^ 0x3f803f80u); } }
    unsigned aK[5];
#pragma unroll
    for (int i = 0; i < 5; i++) { const int row = 8 * q4 + 3 - p4 + (i < 4 ? 4 + 4 * (i >> 1) - 2 * (i & 1) - (g >> 1) : 4 * (g >> 1));
      aK[i] = L + (i < 4 ? A_K : A_V) + row * 256 + ((((row >> 1) & 12) ^ (row & 3) ^ (g & 1) ^ (2 * wv)) << 4); PIN(aK[i]); }
    unsigned ko = WS_KVT + hf * 256 * KS + (255 - 4 * ai - g) * KS + r16 * 16 + wv * 256, hpo = (tid & 255) ? ch0 * 4096 + tid * 16 - 8 : 0xffff0000u;
    PIN(ko); PIN(hpo);
    if (wv == 7) { set_warm(); PIN(va[0]); PIN(va[1]); }
    const int chl = wv < 4 ? wv : 4;
    const float Dc = __uint_as_float((*(const __attribute__((address_space(4))) unsigned*)(AD + ((ch0 + chl) & ~1)) >> (((ch0 + chl) & 1) * 16)) << 16);
    if (tid < 6) *(LAS unsigned*)(size_t)(L + L_DONE + tid * 4) = 0;
    asm volatile("s_waitcnt vmcnt(6)" : "+v"(wk), "+v"(dm), "+v"(idl), "+v"(idh)::"memory");
    BAR();
    u32x4 hv[3]; u32x2 hp[3];
    auto load_h = [&]() {
#pragma unroll
      for (int u = 0; u < 3; u++) if (u < 2 || wv < 4) {
        hv[u] = gld128(rh, tid * 16, ch0 * 4096 + u * 8192);
        hp[u] = __builtin_amdgcn_raw_buffer_load_b64(rh, hpo, u * 8192, 0); } };
    auto warm_wo = [&]() { const i32x4 so_ = make_srd(Bo, 640 * 640 * 2);
      const unsigned r = (lane * 52) >> 8, vq = (r * 1280 + (lane - 5 * r) * 128) | (lane < 50 ? 0u : 0xf0000000u);
#pragma unroll
      for (int i = 0; i < 4; i++) touch(so_, vq, (i * 160 + 10 * j) * 1280 + (xcd & 1) * 640, L + L_DUMMY); };
    auto warm_wq = [&]() { const unsigned r = (lane * 52) >> 8, vq = (r * 3840 + (lane - 5 * r) * 128) | (lane < 50 ? 0u : 0xf0000000u);
#pragma unroll
      for (int i = 0; i < 4; i++) touch(sw, vq, (i * 160 + 10 * j) * 3840 + (xcd & 1) * 640, L + L_DUMMY); };
    if (wv == 7) {
#pragma unroll
      for (int s = P1 + 1; s < 10; s++) warm(s);
      if (WQB == 0) warm_wq();
    }
#pragma unroll
    for (int u = 0; u < 3; u++)
#pragma unroll
      for (int e = 0; e < 2; e++) { const unsigned w = __builtin_amdgcn_ds_bpermute((16 * u + r16) * 4, wk[e]);
        bw[u][e] = dm & __builtin_amdgcn_perm(w, w, g < 2 ? 0x01000100u : 0x03020302u); PIN(bw[u][e]); }
    ST(14);
    if (wv == 7) asm volatile("s_waitcnt vmcnt(%0)" ::"i"((WQB == 0 ? 22 : 18) - 2 * P1) : "memory"); else WAITV(0);
    ST(15); BAR();
    ST(1);
    auto kvt = [&](int nt) {
      u32x4 fr[3][5]; f32x4 k0[3], k1[3], v0[3], v1[3];
#pragma unroll
      for (int u = 0; u < nt; u++)
#pragma unroll
        for (int i = 0; i < 5; i++) { fr[u][i] = ld128(aK[i], (i < 4 ? KP : VP) * u); SCHED(); }
      if (HB == 1) load_h();
      SCHED();
#pragma unroll
      for (int u = 0; u <= nt; u++) {
        if (u < nt) { const f32x4 z = {0, 0, 0, 0};
          k0[u] = mfma16(fr[u][0], bw[u][0], z); SCHED(); k1[u] = mfma16(fr[u][2], bw[u][0], z); SCHED();
          k0[u] = mfma16(fr[u][1], bw[u][1], k0[u]); SCHED(); k1[u] = mfma16(fr[u][3], bw[u][1], k1[u]); SCHED();
          v0[u] = mfma16(fr[u][4], idl, z); SCHED(); v1[u] = mfma16(fr[u][4], idh, z); SCHED(); }
        if (u > 0) { const int w = u - 1;
          gst128c(rws, ko + w * 2048, (u32x4){pk(mul1(k1[w][0], v1[w][0]), mul1(k1[w][1], v1[w][1])), pk(mul1(k1[w][2], v1[w][2]), mul1(k1[w][3], v1[w][3])),
                                              pk(mul1(k0[w][0], v0[w][0]), mul1(k0[w][1], v0[w][1])), pk(mul1(k0[w][2], v0[w][2]), mul1(k0[w][3], v0[w][3]))}); SCHED(); }
      } };
    if (wv < 4) kvt(3); else kvt(2);
    ST(8);
    unsigned xo, xa, ha = L + (tid >> 8) * HCH + 384 + (tid & 255) * 16;
    { const unsigned blk = (tid * 13108) >> 16, ch = tid - 5 * blk; xo = blk * KS + ch * 16; xa = L + A_X + ch * XCH + blk * 16; }
    unsigned aH = L + lw * HCH + (3 - p4) * HCP + (64 * q4 + 8 * g) * 2;
    const int bc = 8 * (r16 & 7) + 4 * (r16 >> 3);
    unsigned aXr = L + A_X + lw * XCH + (1852 - 1792 - bc + 8 * g) * 2;
    unsigned aE = L + A_X + chl * XCH + (2044 - 1792 - 64 * g - bc) * 2;
    unsigned aH5 = aH + A_H4 - lw * HCH + 128 * lw, aX5 = aXr + (4 - lw) * XCH + 128 * lw, aD = L + A_DMP + lw * 2048 + lane * 16, aF = L + L_DONE + lw * 4, aC = L + L_CNT;
    unsigned qo = (tid >> 4) * 1280 + r16 * 16;
    PIN(xo); PIN(xa); PIN(ha); PIN(aH); PIN(aXr); PIN(aE); PIN(aH5); PIN(aX5); PIN(aD); PIN(aF); PIN(aC); PIN(qo);
    auto hcopy = [&](int u0, int u1) {
#pragma unroll
      for (int u = u0; u < u1; u++) if (u < 2 || wv < 4) {
        const unsigned e0 = __builtin_amdgcn_alignbyte(hp[u][1], hp[u][0], 2), e1 = __builtin_amdgcn_alignbyte(hv[u][0], hp[u][1], 2), e2 = __builtin_amdgcn_alignbyte(hv[u][1], hv[u][0], 2),
                       e3 = __builtin_amdgcn_alignbyte(hv[u][2], hv[u][1], 2), e4 = __builtin_amdgcn_alignbyte(hv[u][3], hv[u][2], 2);
        const unsigned ad = u < 2 ? ha : ha + A_H4; const int o = u == 1 ? 2 * HCH : 0;
        st128(ad, o, hv[u]); st128(ad, o + HCP, (u32x4){e1, e2, e3, e4}); st128(ad, o + 2 * HCP, (u32x4){hp[u][1], hv[u][0], hv[u][1], hv[u][2]}); st128(ad, o + 3 * HCP, (u32x4){e0, e1, e2, e3}); } };
    WAITV(0);
    BAR();
    if (wv == 7 && lane == 0) set_flag(ws, FL_K + hf * 64 + ai, tag);
    ST(2);
    auto reset_o = [&]() { if (wv == 5 || wv == 6) { const unsigned c2 = WS_O + (hf * 64 + ai) * OS + (wv - 5) * 10240 + lane * 16;
#pragma unroll
        for (int i = 0; i < 10; i++) gst128c(rws, c2 + i * 1024, emp); } };
    if (OCL == 0) reset_o();
    if (HB == 0) load_h();
    if (HB == 1 && wv < 7) hcopy(0, 3);
    if (wv == 6) { if (WQB == 1) warm_wq(); if (WOT == 0) warm_wo(); }
    if (OCL == 1) reset_o();
    if ((tid & 31) < 24) { st128(L + (tid >> 5) * HCP + (tid & 31) * 16, 0, (u32x4){0, 0, 0, 0}); if (wv < 2) st128(L + A_H4 + (tid >> 5) * HCP + (tid & 31) * 16, 0, (u32x4){0, 0, 0, 0}); }
    else if (tid < 160) st128(L + A_X + (tid >> 5) * XCH + 4096 + (tid & 7) * 16, 0, (u32x4){0, 0, 0, 0});
    if (wv == 7) wait_flags(ws, FL_K + hf * 64 + lane, tag);
    ST(9);
    WAITL0(); BAR();
    ST(3);
    u32x4 F[2][8];
    if (wv < 5) { u32x4 xv[4]; const int xr = ROTO ? ai & 3 : 0;
#pragma unroll
      for (int u = 0; u < 4; u++) xv[u] = gld128c(rws, xo, WS_KVT + hf * 256 * KS + ai * 80 + (u ^ xr) * 64 * KS);
      SCHED();
      if (HB == 0) hcopy(0, 3);
      else if (wv < 4)
#pragma unroll
        for (int t = 0; t < 8; t++) F[0][t] = ld128(aH5, 512 * t);
      SCHED();
#pragma unroll
      for (int u = 0; u < 4; u++) st128(xa + (u ^ xr) * 1024, 0, xv[u]);
    } else {
      if (HB == 0 || wv == 7) hcopy(0, 3);
      if (OCL == 2) reset_o();
      if (WOT == 1 && wv == 7) warm_wo();
    }
    auto resets = [&]() { const unsigned c1 = WS_YT + (ch0 + lw) * YS + lane * 16, c3 = WS_YT + (ch0 + 4) * YS + lw * 1024 + lane * 16;
#pragma unroll
      for (int i = 0; i < 5; i++) gst128c(rws, i < 4 ? c1 + i * 1024 : c3, emp); };
    if (OCL < 3 && (wv == 5 || wv == 6)) WAITV(0);
    const rsrc_t rq = mk(Aq, 2048 * 1280);
    u32x4 qv[3];
    unsigned qo2 = (qo + 512) | (r16 & 8) << 28; PIN(qo2);
    auto load_q = [&]() {
#pragma unroll
      for (int u = 0; u < 3; u++) qv[u] = gld128(rq, u < 2 ? qo + u * 256 : qo2, 32 * ai * 1280 + hf * 640); };
    unsigned yo = WS_YT + (ch0 + chl) * YS + 128 * g + (r16 & 7) * 16 + (r16 >> 3) * 512; PIN(yo);
    auto put_y = [&](f32x4 a, f32x4 c2, unsigned off) {
      const unsigned a0_ = pk(a[0], a[1]), a1_ = pk(a[2], a[3]), b0 = pk(c2[0], c2[1]), b1 = pk(c2[2], c2[3]);
      gst128c(rws, yo + off, (u32x4){(unsigned)__builtin_amdgcn_update_dpp(a0_, b0, 0x128, 0xf, 0xc, false), (unsigned)__builtin_amdgcn_update_dpp(a1_, b1, 0x128, 0xf, 0xc, false),
                                     (unsigned)__builtin_amdgcn_update_dpp(b0, a0_, 0x128, 0xf, 0x3, false), (unsigned)__builtin_amdgcn_update_dpp(b1, a1_, 0x128, 0xf, 0x3, false)}); };
    WAITL0(); BAR();
    ST(4);
    if (wv < 4) {
      f32x4 acc[8], ac5[8];
      u32x2 G[2][8][2], xe[8];
      unsigned cv; f32x4 xs[8];
#pragma unroll
      for (int t = 0; t < 8; t++) { if (HB == 0) F[0][t] = ld128(aH5, 512 * t); G[0][t][0] = ld64(aX5, 512 * (7 - t)); SCHED(); G[0][t][1] = ld64(aX5, 512 * (7 - t) + 8); SCHED(); }
      auto step = [&](f32x4* ac, int cur, unsigned nh, unsigned nx, int off, int mode) {
        int cnt = 0;
#pragma unroll
        for (int t = 0; t < 8; t++)
#pragma unroll
          for (int p = 0; p <= t; p++) {
            const u32x4 gg = cat(G[cur][t - p][0], G[cur][t - p][1]);
            if (p == 0 && (mode & 1)) MFMA_NEW(ac[t], F[cur][p], gg); else MFMA_ACC(ac[t], F[cur][p], gg);
            SCHED();
            if (cnt < 24 && !(mode & 32)) { const int i = cnt / 3, w = cnt % 3;
              if (w == 0) F[1 - cur][i] = ld128(nh, 512 * i + off); else G[1 - cur][i][w - 1] = ld64(nx, 512 * (7 - i) + off + 8 * (w - 1)); }
            if ((mode & 6) && cnt >= 24 && cnt % 3 == 0) { const int d = (cnt - 24) / 3 + (mode & 4); st128(aD, (d >> 1) * 8192 + (d & 1) * 1024, __builtin_bit_cast(u32x4, ac5[d])); }
            if ((mode & 4) && cnt == 35) *(volatile LAS unsigned*)(size_t)aF = 1u;
            if ((mode & 8) && cnt == 25) { aH += 128; PIN(aH); }
            if ((mode & 8) && cnt == 26) { aXr += 128; PIN(aXr); }
            if ((mode & 16) && cnt >= 24 && cnt < 32) xe[cnt - 24] = ld64(aE, 512 * (31 - cnt));
            if ((mode & 16) && cnt == 32) cv = *(volatile LAS unsigned*)(size_t)aC;
            if (mode & 32) {
              const int e = cnt & 3, tt = cnt >> 2;
              if (cnt < 32) { unsigned v = e & 1 ? xe[tt][1 - (e >> 1)] << 16 : xe[tt][1 - (e >> 1)] & 0xffff0000u; PIN(v); xs[tt][e] = __uint_as_float(v); } }
            if (cnt == 34 && !(mode & 32)) { if (mode & 6) WAITL(4); else WAITL0(); }
            cnt++;
            SCHED();
          }
      };
#define STC(n)
      step(ac5, 0, aH5, aX5, 64, 1); STC(13); step(ac5, 1, aH, aXr, 0, 0); STC(14);
      step(acc, 0, aH, aXr, 64, 1 | 2); STC(15); step(acc, 1, aH, aXr, 128, 4 | 8); STC(8);
      load_q(); SCHED();
#pragma unroll 1
      for (int r = 0; r < 2; r++) { step(acc, 0, aH, aXr, 64, 0); step(acc, 1, aH, aXr, 128, 8); }
      step(acc, 0, aH, aXr, 64, 16); step(acc, 1, aH, aXr, 0, 32);
      ST(10);
      if (__builtin_amdgcn_readfirstlane(cv) < 8) wait_cnt(8);
#pragma unroll
      for (int t = 0; t < 8; t += 2) { if (t == 6) asm volatile("" : "+v"(acc[6]), "+v"(acc[7]));
        put_y(acc[t] + Dc * xs[t], acc[t + 1] + Dc * xs[t + 1], 512 * t); SCHED(); }
    } else {
      __builtin_amdgcn_s_setprio(HPRIO);
      if (OCL == 3) { const unsigned c2 = WS_O + (hf * 64 + ai) * OS + lw * 5120 + lane * 16;
#pragma unroll
        for (int i = 0; i < 5; i++) gst128c(rws, c2 + i * 1024, emp);
        WAITV(0); }
      arrive(); wait_cnt(4);
      if (wv == 7 && lane == 0) set_flag(ws, FL_O + hf * 64 + ai, tag);
      resets();
      WAITV(0); arrive(); wait_cnt(8);
      if (wv == 7 && lane == 0) set_flag(ws, FL_A + hf * 64 + ai, tag);
#pragma unroll 1
      for (int it = 0; it < (1 << 20) && !__all(*(volatile LAS unsigned*)(size_t)(L + L_DONE + p4 * 4) == 1u); it++) __builtin_amdgcn_s_sleep(2);
      asm volatile("" ::: "memory");
      { const unsigned rb_ = L + A_DMP + lw * 8192 + lane * 16;
        f32x4 sm[2];
#pragma unroll
        for (int e = 0; e < 2; e++) { sm[e] = __builtin_bit_cast(f32x4, ld128(rb_, e * 1024));
#pragma unroll
          for (int w = 1; w < 4; w++) { const f32x4 o = __builtin_bit_cast(f32x4, ld128(rb_, w * 2048 + e * 1024));
#pragma unroll
            for (int i = 0; i < 4; i++) sm[e][i] = add1(sm[e][i], o[i]); }
          const u32x2 xr = ld64(aE + 512 * (7 - 2 * lw - e), 0);
          sm[e] = (f32x4){fma1(Dc, bhi(xr[1]), sm[e][0]), fma1(Dc, blo(xr[1]), sm[e][1]), fma1(Dc, bhi(xr[0]), sm[e][2]), fma1(Dc, blo(xr[0]), sm[e][3])}; }
        put_y(sm[0], sm[1], 1024 * lw); }
      load_q();
      if (wv == 7) {
#pragma unroll 1
        for (int it = 0; it < (1 << 18); it++) {
          bool ok = ld_flag(ws, FL_A + hf * 64 + lane) == tag;
          if (b == 128) ok = ok & (ld_flag(ws, lane) == tag) & (ld_flag(ws, 64 + lane) == tag) & (ld_flag(ws, 128 + lane) == tag) & (ld_flag(ws, 192 + lane) == tag);
          if (__all(ok)) break;
          __builtin_amdgcn_s_sleep(4);
        }
        asm volatile("" ::: "memory");
        if (b == 128 && lane == 0) __hip_atomic_store((GAS unsigned*)(ws + WS_EPOCH), tag, __ATOMIC_RELAXED, __HIP_MEMORY_SCOPE_AGENT);
      }
      __builtin_amdgcn_s_setprio(0);
    }
    ST(5);
    WAITV(0);
    WAITL0(); BAR();
    if (YD > 0) __builtin_amdgcn_s_sleep(YD > 0 ? YD : 0);
    ST(11);
    { u32x4 yv[3];
      const unsigned yl = (tid >> 2) * YS + p4 * 16, ys = L + T_SCR + p4 * (8 * 640 + 32) + (tid >> 2) * 2;
      yv[2] = (u32x4){0, 0, 0, 0};
#pragma unroll
      for (int u = 0; u < 3; u++) if (u < 2 || wv < 4) yv[u] = gld128c(rws, yl, WS_YT + (320 * hf + 128 * u) * YS + ai * 64);
#pragma unroll 1
      for (int it = 0; it < SWEEPS && !none_empty(mx4(mx4(mx4(0, yv[0]), yv[1]), yv[2])); it++) {
        EV(6);
        asm volatile("" ::: "memory");
#pragma unroll
        for (int u = 0; u < 3; u++) if (mx4(0, yv[u]) == EMPTY) yv[u] = gld128c(rws, yl, WS_YT + (320 * hf + 128 * u) * YS + ai * 64);
      }
      ST(6);
#pragma unroll
      for (int u = 0; u < 3; u++) if (u < 2 || wv < 4) {
#pragma unroll
        for (int e = 0; e < 4; e++) { st16(ys, 256 * u + 1280 * e, (u16)yv[u][e]); st16(ys, 256 * u + 1280 * e + 640, (u16)(yv[u][e] >> 16)); } } }
    WAITL0(); BAR();
    ST(12);
    { const unsigned ya = L + T_SCR + (tid >> 4) * 640 + (wv >> 1) * 32 + r16 * 16, oo = (tid >> 4) * 2560 + r16 * 16 + 32 * ai * 2560 + hf * 640;
      u32x4 y[3];
#pragma unroll
      for (int u = 0; u < 3; u++) y[u] = ld128(ya, 256 * u);
      SCHED();
#pragma unroll
      for (int u = 0; u < 3; u++) { u32x4 r;
#pragma unroll
        for (int e = 0; e < 4; e++) r[e] = pk(blo(y[u][e]) * blo(qv[u][e]), bhi(y[u][e]) * bhi(qv[u][e]));
        __builtin_amdgcn_raw_buffer_store_b128(r, ro, u < 2 ? oo + u * 256 : (oo + 512) | (r16 & 8) << 28, 0, OUTA); } }
  }
  ST(7); STEND();
}

extern "C" void kernel_launch(void* const* d_in, const int* in_sizes, int n_in, void* d_out, int out_size, void* d_ws, size_t ws_size, hipStream_t stream) {
  fused_kernel<<<256, 512, 0, stream>>>((const u16*)d_in[0], (const u16*)d_in[1], (const u16*)d_in[2], (const u16*)d_in[3], (const u16*)d_in[4], (const u16*)d_in[5],
                                        (const u16*)d_in[6], (const u16*)d_in[7], (const u16*)d_in[8], (u16*)d_out, (char*)d_ws);
}
```

```cpp
#include <hip/hip_runtime.h>
#include <hip/hip_bf16.h>

typedef unsigned short u16;
typedef unsigned long long u64;
typedef short s16x4 __attribute__((ext_vector_type(4)));
typedef __bf16 bf16x2 __attribute__((ext_vector_type(2)));
typedef __bf16 bf16x8 __attribute__((ext_vector_type(8)));
typedef float f32x2 __attribute__((ext_vector_type(2)));
typedef float f32x4 __attribute__((ext_vector_type(4)));
typedef unsigned u32x2 __attribute__((ext_vector_type(2)));
typedef unsigned u32x4 __attribute__((ext_vector_type(4)));
typedef int i32x4 __attribute__((ext_vector_type(4)));
#define LAS __attribute__((address_space(3)))
#define GAS __attribute__((address_space(1)))
#define DI __device__ __forceinline__
#define WAITV(n) asm volatile("s_waitcnt vmcnt(" #n ")" ::: "memory")
#define WAITL0() __builtin_amdgcn_s_waitcnt(0xC07F)
#define WAITL(n) __builtin_amdgcn_s_waitcnt(0xC07F | ((n) << 8))
#define BAR() do { asm volatile("" ::: "memory"); __builtin_amdgcn_s_barrier(); asm volatile("" ::: "memory"); } while (0)
#define SCHED() __builtin_amdgcn_sched_barrier(0)
#define PIN(x) asm volatile("" : "+v"(x))
constexpr unsigned CODE_BYTES = 36128;

DI f32x4 mfma16(u32x4 a, u32x4 b, f32x4 c) { return __builtin_amdgcn_mfma_f32_16x16x32_bf16(__builtin_bit_cast(bf16x8, a), __builtin_bit_cast(bf16x8, b), c, 0, 0, 0); }
DI u32x4 cat(u32x2 a, u32x2 b) { return __builtin_shufflevector(a, b, 0, 1, 2, 3); }
DI u32x4 ld128(unsigned a, int off) { return *(const LAS u32x4*)((LAS char*)(size_t)a + off); }
DI u32x2 ld64(unsigned a, int off) { return *(const LAS u32x2*)((LAS char*)(size_t)a + off); }
DI u32x2 ldtr(unsigned a, int off) { return __builtin_bit_cast(u32x2, __builtin_amdgcn_ds_read_tr16_b64_v4i16((LAS s16x4*)((LAS char*)(size_t)a + off))); }
DI void st128(unsigned a, int off, u32x4 v) { *(LAS u32x4*)((LAS char*)(size_t)a + off) = v; }
DI void st64(unsigned a, int off, u32x2 v) { *(LAS u32x2*)((LAS char*)(size_t)a + off) = v; }
DI void st16(unsigned a, int off, u16 v) { *(LAS u16*)((LAS char*)(size_t)a + off) = v; }
DI unsigned pk(float lo, float hi) { f32x2 v = {lo, hi}; return __builtin_bit_cast(unsigned, __builtin_convertvector(v, bf16x2)); }
DI float blo(unsigned u) { return __uint_as_float(u << 16); }
DI float bhi(unsigned u) { return __uint_as_float(u & 0xffff0000u); }
constexpr float LOG2E = 1.44269504088896341f;
DI float add1(float a, float b) { float r = a + b; asm("" : "+v"(r)); return r; }
DI float mul1(float a, float b) { float r = a * b; asm("" : "+v"(r)); return r; }
DI float fma1(float a, float b, float c) { float r = __builtin_fmaf(a, b, c); asm("" : "+v"(r)); return r; }
#define MFMA_NEW(acc, a, b) asm("v_mfma_f32_16x16x32_bf16 %0, %1, %2, 0" : "=&v"(acc) : "v"(a), "v"(b))
#define MFMA_ONE(acc, a, b) asm("v_mfma_f32_16x16x32_bf16 %0, %1, %2, 1.0" : "=&v"(acc) : "v"(a), "v"(b))
#define MFMA_INI(acc, a, b, c) asm("v_mfma_f32_16x16x32_bf16 %0, %1, %2, %3" : "=&v"(acc) : "v"(a), "v"(b), "v"(c))
#define MFMA_ACC(acc, a, b) asm("v_mfma_f32_16x16x32_bf16 %0, %1, %2, %0" : "+v"(acc) : "v"(a), "v"(b))
DI i32x4 make_srd(const void* p, unsigned bytes) { u64 a = (u64)p; i32x4 r; r[0] = (int)(unsigned)a; r[1] = (int)(unsigned)(a >> 32) & 0xffff; r[2] = (int)bytes; r[3] = 0x00020000; return r; }
DI void dma16(i32x4 srd, unsigned voff, unsigned soff, unsigned m0v) {
  asm volatile("s_mov_b32 m0, %3\n\ts_nop 0\n\tbuffer_load_dwordx4 %0, %1, %2 offen lds" ::"v"(voff), "s"(srd), "s"(soff), "s"(m0v) : "memory");
}
DI void dma16x3(i32x4 srd, unsigned v0, unsigned v1, unsigned v2, unsigned soff, unsigned m0v) {
  asm volatile("s_mov_b32 m0, %5\n\ts_nop 0\n\tbuffer_load_dwordx4 %0, %3, %4 offen lds\n\tbuffer_load_dwordx4 %1, %3, %4 offen offset:1024 lds\n\tbuffer_load_dwordx4 %2, %3, %4 offen offset:2048 lds"
               ::"v"(v0), "v"(v1), "v"(v2), "s"(srd), "s"(soff), "s"(m0v) : "memory");
}
DI void dma16x4(i32x4 srd, unsigned v0, unsigned v1, unsigned v2, unsigned v3, unsigned soff, unsigned m0v) {
  asm volatile("s_mov_b32 m0, %6\n\ts_nop 0\n\tbuffer_load_dwordx4 %0, %4, %5 offen lds\n\tbuffer_load_dwordx4 %1, %4, %5 offen offset:1024 lds\n\t"
               "buffer_load_dwordx4 %2, %4, %5 offen offset:2048 lds\n\tbuffer_load_dwordx4 %3, %4, %5 offen offset:3072 lds"
               ::"v"(v0), "v"(v1), "v"(v2), "v"(v3), "s"(srd), "s"(soff), "s"(m0v) : "memory");
}
DI void touch(i32x4 srd, unsigned voff, unsigned soff, unsigned m0v) {
  asm volatile("s_mov_b32 m0, %3\n\ts_nop 0\n\tbuffer_load_dword %0, %1, %2 offen lds" ::"v"(voff), "s"(srd), "s"(soff), "s"(m0v) : "memory");
}
typedef __amdgpu_buffer_rsrc_t rsrc_t;
DI rsrc_t mk(const void* p, unsigned bytes) { return __builtin_amdgcn_make_buffer_rsrc((void*)p, 0, bytes, 0x00020000); }
constexpr int SC1 = 16;
DI u32x4 gld128(rsrc_t r, unsigned v, unsigned s) { return __builtin_amdgcn_raw_buffer_load_b128(r, v, s, 0); }
DI u32x4 gld128c(rsrc_t r, unsigned v, unsigned s) { return __builtin_amdgcn_raw_buffer_load_b128(r, v, s, SC1); }
DI void gst128c(rsrc_t r, unsigned v, u32x4 d) { __builtin_amdgcn_raw_buffer_store_b128(d, r, v, 0, SC1); }

constexpr unsigned WS_EPOCH = 0;
constexpr unsigned WS_FLAG = 4096;
constexpr unsigned FL_G = 0;
constexpr unsigned FL_K = 128;
constexpr unsigned FL_A = 256;
constexpr unsigned FL_O = 384;
constexpr unsigned PRS = 4480, YS = 4480, KS = 5248, OS = 20864;
constexpr unsigned WS_PART = 1u << 18;
constexpr unsigned WS_O = 1u << 20;
constexpr unsigned WS_KVT = 4u << 20;
constexpr unsigned WS_YT = 8u << 20;
constexpr unsigned WS_BYTES = 16u << 20;

DI void set_flag(char* ws, unsigned idx, u64 tag) { __hip_atomic_store((GAS u64*)(ws + WS_FLAG + idx * 128), tag, __ATOMIC_RELAXED, __HIP_MEMORY_SCOPE_AGENT); }
DI void wait_flags(char* ws, unsigned idx, u64 tag) {
  GAS u64* p = (GAS u64*)(ws + WS_FLAG + idx * 128);
#pragma unroll 1
  for (int spins = 0; spins < (1 << 18); ++spins) {
    const u64 v = __hip_atomic_load(p, __ATOMIC_RELAXED, __HIP_MEMORY_SCOPE_AGENT);
    if (__all(v == tag)) break;
    __builtin_amdgcn_s_sleep(1);
  }
  asm volatile("" ::: "memory");
}
DI u64 ld_flag(char* ws, unsigned idx) { return __hip_atomic_load((GAS u64*)(ws + WS_FLAG + idx * 128), __ATOMIC_RELAXED, __HIP_MEMORY_SCOPE_AGENT); }
constexpr unsigned EMPTY = 0xffffffffu;
constexpr int SWEEPS = 1 << 16;
DI unsigned umax3(unsigned a, unsigned b, unsigned c) { return __builtin_elementwise_max(__builtin_elementwise_max(a, b), c); }
DI unsigned mx4(unsigned m, u32x4 v) { return umax3(umax3(m, v[0], v[1]), v[2], v[3]); }
DI bool none_empty(unsigned m) { return __builtin_amdgcn_ballot_w64(m == EMPTY) == 0; }

constexpr int SLOT = 40960, XB = 16384, WB = 12288;
constexpr int G_RED = 36864;
constexpr int L_DONE = 161792;
constexpr int L_CNT = 161808;
constexpr int L_PAIR = 161824;
constexpr int L_DUMMY = 163328;
constexpr int WOP[4] = {131072, 45056, 81920, 0};
constexpr int G_OUT = 112640;
constexpr int HCP = 4640, HCH = 4 * HCP;
constexpr int A_X = 4 * HCH, XCH = 4224;
constexpr int A_H4 = A_X + 5 * XCH;
constexpr int KP = 36 * 256, VP = 32 * 256;
constexpr int A_K = A_X, A_V = A_X + 3 * KP;
constexpr int A_DMP = 113920;
constexpr int T_SCR = 61440;

__global__ void __launch_bounds__(512, 2)
fused_kernel(const u16* __restrict__ Aq, const u16* __restrict__ Ak, const u16* __restrict__ Av, const u16* __restrict__ Ask,
             const u16* __restrict__ Ah, const u16* __restrict__ AD, const u16* __restrict__ Bx, const u16* __restrict__ Bw,
             const u16* __restrict__ Bo, u16* __restrict__ out, char* __restrict__ ws) {
  __shared__ __attribute__((aligned(16))) char smem[163840];
  const int b = blockIdx.x, xcd = b & 7, j = (b >> 3) & 15;
  int role = b >> 7;
  const int tid = threadIdx.x, lane = tid & 63;
  int wv = __builtin_amdgcn_readfirstlane(tid >> 6);
  const unsigned L = (unsigned)(size_t)(LAS char*)smem;
  {
    u64 pc; asm volatile("s_getpc_b64 %0" : "=s"(pc));
    touch(make_srd((const void*)(pc & ~127ull), CODE_BYTES), (tid * 32 + (b >> 3)) * 128, 0, L + L_DUMMY); }
  asm volatile("" ::"s"(Aq), "s"(Ak), "s"(Av), "s"(Ask), "s"(Ah), "s"(AD), "s"(Bx), "s"(Bw), "s"(Bo), "s"(out), "s"(ws));
  const int rb = (xcd >> 1) * 4 + (j >> 2), cg = (xcd & 1) * 4 + (j & 3);
  const int a0 = (160 * cg) & ~63, dl = 160 * cg - a0;
  const int hf = xcd & 1, ai = (xcd >> 1) * 16 + j;
  const i32x4 sx = make_srd(Bx, 2048 * 640 * 2), sw = make_srd(Bw, 640 * 1920 * 2);

  unsigned va[3], vb[3], vx[4];
  int sp = 0;
  const int wx = wv & 3;
  u32x2 wk;
  auto issue_x = [&](int slot) { dma16x4(sx, vx[0], vx[1], vx[2], vx[3], sp * 128, L + slot * SLOT + wx * 4096); };
  auto issue_w = [&](int slot) {
    const int lw = wx; const unsigned lb = L + slot * SLOT, so = (sp * 64 + 16 * lw) * 3840 + 1280 + a0;
    dma16x3(sw, va[0], va[1], va[2], so, lb + XB + lw * 3072);
    dma16x3(sw, vb[0], vb[1], vb[2], so + 1280, lb + XB + WB + lw * 3072);
    sp++;
  };
  auto set_warm = [&]() { const unsigned r = (lane * 26) >> 8, l = lane - r * 10;
    va[0] = (((xcd >> 1) * 512 + 32 * j + lane) * 1280) | (lane < 32 ? 0u : 0xf0000000u);
    va[1] = ((4 * j + r) * 3840 + (l >= 5 ? 640 : 0) + l * 128) | (lane < 40 ? 0u : 0xf0000000u); };
  auto warm = [&](int s) { touch(sx, va[0], s * 128, L + L_DUMMY); touch(sw, va[1], s * 64 * 3840 + 1280 + (xcd & 1) * 640, L + L_DUMMY); };
  const rsrc_t rws = mk(ws, WS_BYTES);
  const u64 epoch = *(const __attribute__((address_space(4))) u64*)(ws + WS_EPOCH);
  if (role == 0) {
    if (wv < 4) {
      vx[0] = (rb * 128 + 32 * wx + (lane >> 3)) * 1280 + (((lane & 7) ^ (lane >> 4)) << 4);
      vx[1] = (vx[0] ^ 64) + 10240 - 1024; vx[2] = vx[0] + 2 * (10240 - 1024); vx[3] = vx[1] + 2 * (10240 - 1024);
      SCHED();
      issue_x(0);
#pragma unroll
      for (int u = 0; u < 3; u++) { const unsigned c = u * 64 + lane, row = (c * 171) >> 11, pos = c - row * 12; va[u] = row * 3840 + ((pos ^ ((row >> 3) << 1)) << 4) - 1024 * u; vb[u] = va[u] ^ 16; }
      issue_w(0);
    }
  } else {
    const i32x4 sk_ = make_srd(Ak, 2048 * 1280), sv_ = make_srd(Av, 2048 * 1280);
    const unsigned rl = lane >> 4, base = (32 * ai + 4 * wv + rl) * 1280 + hf * 640;
    const unsigned ck = ((lane ^ rl ^ (((wv + 1) & 6) << 1)) & 15) << 4, cv = ((lane ^ rl ^ ((wv & 6) << 1)) & 15) << 4;
    if (wv < 7) { set_warm(); warm(wv); }
    asm volatile("global_load_dwordx2 %0, %1, %2" : "=v"(wk) : "v"((320 * hf + 16 * wv + (rl < 2 || (rl == 2 && wv < 4) ? 128 * rl : 0) + (lane & 15)) * 8), "s"(Ask) : "memory");
#pragma unroll
    for (int u = 0; u < 3; u++) {
      dma16(sk_, (base + ck) | (u == 2 ? (ck & 128) << 24 : 0), u * 256, L + A_K + u * KP + (wv + 1) * 1024);
      dma16(sv_, (base + cv) | (u == 2 ? (cv & 128) << 24 : 0), u * 256, L + A_V + u * VP + wv * 1024); }
    if (wv < 3) { const unsigned c0 = ((lane ^ rl) & 15) << 4;
      dma16(sk_, ((32 * ai - 4 + rl) * 1280 + hf * 640 + c0 + wv * 256) | (wv == 2 ? (c0 & 128) << 24 : 0), 0, L + A_K + wv * KP); }
    SCHED();
  }
  u64 tag = wv == 7 ? epoch + 1 : 0;
  asm volatile("" : "+s"(role), "+s"(wv), "+s"(tag));
  const int lw = wv & 3, g = lane >> 4, r16 = lane & 15, q4 = r16 >> 2, p4 = lane & 3;
  const rsrc_t ro = mk(out, 2048 * 1280 * 2);
  auto wait_cnt = [&](unsigned need) {
#pragma unroll 1
    for (int it = 0; it < (1 << 20) && __builtin_amdgcn_readfirstlane(*(volatile LAS unsigned*)(size_t)(L + L_CNT)) < need; it++) __builtin_amdgcn_s_sleep(1);
    asm volatile("" ::: "memory"); };
  auto arrive = [&]() { if (lane == 0) __hip_atomic_fetch_add((LAS unsigned*)(size_t)(L + L_CNT), 1u, __ATOMIC_RELAXED, __HIP_MEMORY_SCOPE_WORKGROUP); };
  const u32x4 emp = {EMPTY, EMPTY, EMPTY, EMPTY};

  if (role == 0) {
    const int rt0 = rb * 8;
    const i32x4 so_ = make_srd(Bo, 640 * 640 * 2);
    const int h1 = xcd & 1;
    unsigned vo[3];
    int gi = lw;
    auto set_vo = [&]() {
#pragma unroll
      for (int u = 0; u < 3; u++) { const unsigned cc = u * 64 + lane, row = (cc * 171) >> 11, ps = cc - row * 12; vo[u] = row * 1280 + ((ps ^ (((row >> 2) & 1) << 1)) << 4) - 1024 * u; PIN(vo[u]); } };
    auto prog = [&]() { return (unsigned)__builtin_amdgcn_readfirstlane(*(volatile LAS unsigned*)(size_t)(L + L_DONE)); };
    auto wo_groups = [&](int n, int end) {
#pragma unroll 1
      for (; n > 0 && gi < end; n--, gi += 4) { const int pos = (gi < 10 ? WOP[0] : gi < 20 ? WOP[1] - 30720 : gi < 30 ? WOP[2] - 61440 : WOP[3] - 92160) + gi * 3072;
        dma16x3(so_, vo[0], vo[1], vo[2], (gi + (h1 ? (gi < 20 ? 20 : -20) : 0)) * 16 * 1280 + a0, L + pos); } };
    if (wv < 4) {
      auto issue_q = [&](int pos) {
        const unsigned so = ((sp - 10) * 64 + 16 * lw) * 3840 + a0;
        dma16x3(sw, va[0], va[1], va[2], so, L + pos + lw * 3072);
        sp++;
      };
      sp = 1;
      PIN(vb[0]); PIN(vb[1]); PIN(vb[2]); PIN(vx[1]); PIN(vx[2]); PIN(vx[3]);
      issue_x(1); issue_w(1);
      SCHED();
      unsigned aX[2]; { const int row = lw * 32 + r16;
#pragma unroll
        for (int kk = 0; kk < 2; kk++) aX[kk] = L + row * 128 + (((4 * kk + g) ^ ((row >> 1) & 7)) << 4); }
      u32x4 xq[10][2][2];
      constexpr int QP[10] = {45056, 45056 + WB, 45056 + 2 * WB, 81920, 81920 + WB, 81920 + 2 * WB, 81920 + 3 * WB, 0, WB, 2 * WB};
      unsigned aQ[5], aE[6], o16[2], o8, aP;
      __builtin_amdgcn_s_setprio(3);
#pragma unroll
      for (int s = 0; s < 10; s++) {
        if (s < 9) WAITV(10); else WAITV(9);
        WAITL0(); BAR();
        if (s < 8) issue_x((s + 2) % 3); else issue_q(QP[s == 8 ? 0 : 3]);
#pragma unroll
        for (int kk = 0; kk < 2; kk++)
#pragma unroll
          for (int m = 0; m < 2; m++) xq[s][kk][m] = ld128(aX[kk] + (s % 3) * SLOT, m * 2048);
        if (s == 0) { int ln = lane; PIN(ln); const int lane = ln, g = ln >> 4, r16 = ln & 15, q4 = r16 >> 2, p4 = ln & 3;
#pragma unroll
          for (int n = 0; n < 5; n++) { const int off = (n < 4 ? dl + 40 * n + 8 * p4 : dl + 40 * p4 + 32) ^ ((g & 1) << 5); aQ[n] = L + QP[0] + (8 * g + q4) * 192 + off; PIN(aQ[n]); }
          { const int col = p4 == 0 ? 8 + q4 : 0; aE[0] = L + G_RED + lane * 16; aE[1] = L + G_RED + r16 * 16; aE[2] = L + G_RED + (16 * g + col) * 16; aE[3] = L + G_RED + col * 16;
            aE[4] = aE[2] ^ 32; aE[5] = aE[3] ^ 32; }
          const unsigned ob = WS_O + (rb * 8 + lw * 2) * OS + (cg >> 1) * 5120;
          o16[0] = ob + (cg & 1) * 2048 + lane * 16; o16[1] = o16[0] + OS; o8 = ob + 4096 + (cg & 1) * OS + lane * 16;
          PIN(aE[0]); PIN(aE[1]); PIN(aE[2]); PIN(aE[3]); PIN(aE[4]); PIN(aE[5]); PIN(o16[0]); PIN(o16[1]); PIN(o8);
          set_vo();
          aP = L + (lane ? L_DUMMY + lane * 4 : L_DONE); PIN(aP);
        }
        if (s < 8) issue_w((s + 2) % 3);
        else if (s == 8) { issue_q(QP[1]); issue_q(QP[2]); }
        else { issue_q(QP[4]); issue_q(QP[5]); }
      }
      __builtin_amdgcn_s_setprio(0);
      WAITV(9);
      WAITL0(); BAR();
      issue_q(QP[6]); issue_q(QP[7]); issue_q(QP[8]); issue_q(QP[9]);
      f32x4 qa[2][5];
      SCHED();
      {
        u32x2 fw[2][5][2];
        auto rdw = [&](int ks, int n, int h) {
          if (h == 0 && ks == 6) { aQ[n] += QP[3] - QP[0]; asm volatile("" : "+v"(aQ[n])); }
          if (h == 0 && ks == 14) { aQ[n] -= QP[3]; asm volatile("" : "+v"(aQ[n])); }
          fw[ks & 1][n][h] = ldtr(aQ[n], (ks - (ks < 6 ? 0 : ks < 14 ? 6 : 14)) * 32 * 192 + 768 * h); };
#pragma unroll
        for (int n = 0; n < 5; n++) { rdw(0, n, 0); rdw(0, n, 1); }
        SCHED();
#pragma unroll
        for (int ks = 0; ks < 20; ks++) {
          if (ks == 4) { WAITV(0); asm volatile("ds_add_u32 %0, %1 offset:4" ::"v"(aP), "v"(1u) : "memory"); SCHED(); }
          if (ks == 5) { SCHED();
#pragma unroll 1
            for (int it = 0; it < (1 << 22) && __builtin_amdgcn_readfirstlane(*(volatile LAS unsigned*)((LAS char*)(size_t)aP + 4)) < 4; it++) {}
            asm volatile("" ::: "memory"); SCHED(); }
          if (ks == 5 || ks == 19) { asm volatile("ds_add_u32 %0, %1" ::"v"(aP), "v"(1u << (ks == 5 ? 0 : 16)) : "memory"); SCHED(); }
#pragma unroll
          for (int n = 0; n < 5; n++) {
            if (n == 0) WAITL(4); else if (n == 3) WAITL(6);
            SCHED();
            const u32x4 w = cat(fw[ks & 1][n][0], fw[ks & 1][n][1]);
            if (ks) MFMA_ACC(qa[0][n], w, xq[ks >> 1][ks & 1][0]); else MFMA_ONE(qa[0][n], w, xq[0][0][0]);
            SCHED();
            if (ks < 19) rdw(ks + 1, n, 0);
            SCHED();
            if (ks) MFMA_ACC(qa[1][n], w, xq[ks >> 1][ks & 1][1]); else MFMA_ONE(qa[1][n], w, xq[0][0][1]);
            SCHED();
            if (ks < 19) rdw(ks + 1, n, 1);
            SCHED();
          }
        }
        asm volatile("s_nop 7" : "+v"(qa[0][4]), "+v"(qa[1][4]), "+v"(qa[0][3]), "+v"(qa[1][3]), "+v"(qa[0][2]), "+v"(qa[1][2]), "+v"(qa[0][1]), "+v"(qa[1][1]), "+v"(qa[0][0]), "+v"(qa[1][0]));
      }
      SCHED();
      const unsigned pq_ = *(volatile LAS unsigned*)(size_t)aP;
      gi = 20 + lw;
      SCHED();
      u32x4 bq[2][4];
      u32x2 e0[4][2], e1[4][2];
      f32x4 n0[2][4], n1[2][4]; float rc[2][4];
      auto omul = [&](int m, int i) { const int h = i & 3;
        if (i < 4) n1[m][h] = mfma16(cat(e1[h][0], e1[h][1]), bq[m][h], (f32x4){0, 0, 0, 0}); else n0[m][h] = mfma16(cat(e0[h][0], e0[h][1]), bq[m][h], (f32x4){0, 0, 0, 0});
        SCHED(); };
      auto get_state = [&]() {
        SCHED(); BAR(); SCHED();
#pragma unroll
        for (int h = 0; h < 4; h++) { e1[h][0] = ld64(aE[h < 2 ? 2 : 4], (h >> 1) * 2048 + (h & 1) * 64); SCHED(); e1[h][1] = ld64(aE[h < 2 ? 3 : 5], (h >> 1) * 2048 + (h & 1) * 320 + 520); SCHED(); }
#pragma unroll
        for (int h = 0; h < 4; h++) { e0[h][0] = ld64(aE[0], 4096 + (h >> 1) * 2048 + 8 * (h & 1)); SCHED(); e0[h][1] = ld64(aE[1], (h >> 1) * 2048 + (h & 1) * 256 + 8); SCHED(); } };
#pragma unroll
      for (int m = 0; m < 2; m++) {
        f32x2 u[10], a[10];
#pragma unroll
        for (int i = 0; i < 10; i++) { u[i] = (f32x2){qa[m][i >> 1][2 * (i & 1)], qa[m][i >> 1][2 * (i & 1) + 1]}; a[i] = u[i] * LOG2E - LOG2E; }
        SCHED();
        if (m == 0 && (!(__builtin_amdgcn_readfirstlane(pq_) & 0x040000u))) {
#pragma unroll 1
          for (int it = 0; it < (1 << 22) && !(prog() & 0x040000u); it++) {}
          asm volatile("" ::: "memory"); }
        wo_groups(1, 40);
        SCHED();
#pragma unroll
        for (int i = 0; i < 10; i++) { a[i] = (f32x2){__builtin_amdgcn_exp2f(a[i][0]), __builtin_amdgcn_exp2f(a[i][1])}; SCHED(); }
        wo_groups(1, 40);
        SCHED();
#pragma unroll
        for (int i = 0; i < 10; i++) a[i] = (f32x2){__builtin_amdgcn_fmed3f(u[i][0], a[i][0], 1.f), __builtin_amdgcn_fmed3f(u[i][1], a[i][1], 1.f)};
        SCHED();
#pragma unroll
        for (int i = 0; i < 10; i++) a[i] += 1e-6f;
        const unsigned t0 = pk(a[8][0], a[8][1]), t1 = pk(a[9][0], a[9][1]);
#pragma unroll
        for (int h = 0; h < 4; h++) { bq[m][h] = (u32x4){pk(a[2 * h][0], a[2 * h][1]), pk(a[2 * h + 1][0], a[2 * h + 1][1]), g == h ? t0 : 0u, g == h ? t1 : 0u}; PIN(bq[m][h]); }
        SCHED();
        if (m) wo_groups(10, 40);
      }
      get_state();
#pragma unroll
      for (int i = 0; i < 8; i++) omul(0, i);
      const unsigned pw = *(volatile LAS unsigned*)(size_t)aP;
      SCHED();
#pragma unroll
      for (int i = 0; i < 8; i++) { omul(1, i); if (i & 1) rc[0][i >> 1] = __builtin_amdgcn_rcpf(rc[0][i >> 1]); else rc[0][i >> 1] = add1(n1[0][i >> 1][1], 1e-6f); SCHED(); }
      WAITV(0);
      if (!(__builtin_amdgcn_readfirstlane(pw) & 0x8000)) {
#pragma unroll 1
        for (int it = 0; it < (1 << 22) && !(__builtin_amdgcn_readfirstlane(*(volatile LAS unsigned*)(size_t)aP) & 0x8000); it++) __builtin_amdgcn_s_sleep(1);
        asm volatile("" ::: "memory"); }
#pragma unroll
      for (int m = 0; m < 2; m++) {
        if (m)
#pragma unroll
          for (int h = 0; h < 4; h++) rc[1][h] = __builtin_amdgcn_rcpf(n1[1][h][1] + 1e-6f);
#pragma unroll
        for (int hp_ = 0; hp_ < 2; hp_++) { const f32x4 a = n0[m][2 * hp_] * rc[m][2 * hp_], c2 = n0[m][2 * hp_ + 1] * rc[m][2 * hp_ + 1];
          gst128c(rws, o16[m] + hp_ * 1024, (u32x4){pk(a[0], a[1]), pk(a[2], a[3]), pk(c2[0], c2[1]), pk(c2[2], c2[3])}); SCHED(); }
      }
      gst128c(rws, o8, (u32x4){pk(mul1(n1[0][0][0], rc[0][0]), mul1(n1[0][1][0], rc[0][1])), pk(mul1(n1[0][2][0], rc[0][2]), mul1(n1[0][3][0], rc[0][3])),
                               pk(mul1(n1[1][0][0], rc[1][0]), mul1(n1[1][1][0], rc[1][1])), pk(mul1(n1[1][2][0], rc[1][2]), mul1(n1[1][3][0], rc[1][3]))});
      asm volatile("ds_add_u32 %0, %1 offset:16" ::"v"(aP), "v"(1u) : "memory");
      SCHED();
    } else {
      const int rg = lw >> 1, c = lw & 1;
      unsigned aA[2]; { const int row = rg * 64 + r16;
#pragma unroll
        for (int kk = 0; kk < 2; kk++) aA[kk] = L + row * 128 + (((4 * kk + g) ^ ((row >> 1) & 7)) << 4); }
      unsigned aB[5];
#pragma unroll
      for (int n = 0; n < 5; n++) { int sec, off; if (n < 4) { sec = n >> 1; off = dl + 80 * c + 40 * (n & 1) + 8 * p4; } else { sec = p4 >> 1; off = dl + 80 * c + 40 * (p4 & 1) + 32; }
        off ^= ((g & 1) << 5) ^ (sec << 4); aB[n] = L + XB + sec * WB + (8 * g + q4) * 192 + off; }
      if (lane < 12) *(LAS unsigned*)(size_t)(L + L_DONE + lane * 4) = 0;
      f32x4 acc[4][5];
      u32x4 fa[2][4]; u32x2 fb[2][5][2];
      const float i4 = r16 < 8 ? 1.f : 0.f;
      f32x4 c4 = {i4, i4, i4, i4}; PIN(c4);
      unsigned aA2[2], aB2[5];
#pragma unroll
      for (int i = 0; i < 2; i++) { aA2[i] = aA[i] + 2 * SLOT; PIN(aA[i]); PIN(aA2[i]); }
#pragma unroll
      for (int i = 0; i < 5; i++) { aB2[i] = aB[i] + 2 * SLOT; PIN(aB[i]); PIN(aB2[i]); }
      auto rd = [&](int t, int slot, int kk) {
        const int o = slot == 1 ? SLOT : 0;
        if (t < 4) fa[kk][t] = ld128(slot == 2 ? aA2[kk] : aA[kk], o + t * 2048);
        else { const int n2 = (t - 4) >> 1, h = (t - 4) & 1; fb[kk][n2][h] = ldtr(slot == 2 ? aB2[n2] : aB[n2], o + kk * 32 * 192 + h * 768); } };
      auto half = [&](int slot, int kk, bool first = false) {
#pragma unroll
        for (int t = 0; t < 20; t++) {
          const u32x4 w = cat(fb[1 - kk][t >> 2][0], fb[1 - kk][t >> 2][1]);
          if (!first) MFMA_ACC(acc[t & 3][t >> 2], fa[1 - kk][t & 3], w);
          else if (t < 8) MFMA_ONE(acc[t & 3][t >> 2], fa[1 - kk][t & 3], w);
          else if (t < 16) MFMA_NEW(acc[t & 3][t >> 2], fa[1 - kk][t & 3], w);
          else MFMA_INI(acc[t & 3][t >> 2], fa[1 - kk][t & 3], w, c4);
          SCHED();
          if (t < 14) rd(t, slot, kk);
          if (t == 18) WAITL0();
          SCHED();
        }
      };
      unsigned pvo = g * PRS + r16 * 16, aS = L + G_RED + (lw ^ 2) * 2048 + ((g >> 1) * 16 + r16) * 16 + (g & 1) * 8;
      u32x4 sel[2];
      set_vo();
#pragma unroll
      for (int o = 0; o < 2; o++) {
#pragma unroll
        for (int i = 0; i < 4; i++) sel[o][i] = ((r16 >> 1) == 4 * o + i) ? 0x3f80u << ((r16 & 1) * 16) : 0u;
        PIN(sel[o]); }
      PIN(pvo); PIN(aS);
      const unsigned fk = FL_K + hf * 64 + (r16 >> 2) * 16 + (r16 & 3) * 4 + (cg & 3);
      u64 fv = 0;
      __builtin_amdgcn_s_setprio(2);
      WAITL0(); BAR(); SCHED();
#pragma unroll
      for (int t = 0; t < 14; t++) { rd(t, 0, 0); SCHED(); }
      WAITL0(); SCHED();
      half(0, 1, true);
#pragma unroll 1
      for (int s = 0; s < 3; s++) {
        BAR(); SCHED(); half(1, 0); half(1, 1);
        BAR(); SCHED(); half(2, 0); half(2, 1);
        BAR(); SCHED(); half(0, 0); half(0, 1);
      }
      BAR(); SCHED();
      const int irow = c * 2 + rg;
      SCHED();
      const unsigned fo = FL_O + rt0 + (lane & 7);
      if (wv == 7) fv = ld_flag(ws, fk);
      SCHED();
      u32x4 T[2][6]; f32x4 R[7];
#pragma unroll
      for (int t = 0; t < 20; t++) { const int nn = t >> 2, n = nn < 3 ? nn + 2 : nn - 3, m = t & 3;
        const u32x4 w = cat(fb[1][n][0], fb[1][n][1]);
        MFMA_ACC(acc[m][n], fa[1][m], w); SCHED();
        if (t >= 8) {
#pragma unroll
          for (int z = 0; z < 2; z++) { const int i = 2 * (t - 8) + z, tn = i >> 3, tm = (i >> 1) & 3;
            const unsigned v = pk(acc[tm][tn + 2][2 * z], acc[tm][tn + 2][2 * z + 1]);
            T[tm >> 1][tn < 2 ? tn + 2 : 5][2 * (tm & 1) + z] = v; }
          SCHED(); } }
#pragma unroll
      for (int i = 0; i < 8; i++) T[i >> 2][5][i & 3] = r16 < 8 ? 0x3f803f80u : T[i >> 2][5][i & 3];
      SCHED();
      auto rmul = [&](int ks, int i) { constexpr int ta[7] = {0, 1, 0, 1, 4, 4, 4}, tb[7] = {2, 3, 5, 5, 2, 3, 5};
        R[i] = mfma16(T[ks][ta[i]], T[ks][tb[i]], ks ? R[i] : (f32x4){0, 0, 0, 0}); };
#pragma unroll
      for (int ks = 0; ks < 2; ks++) {
        f32x2 u[12], a[12];
#pragma unroll
        for (int i = 0; i < 12; i++) { const int n = i < 4 ? 4 : (i >> 2) - 1, m = 2 * ks + ((i >> 1) & 1);
          u[i] = (f32x2){acc[m][n][2 * (i & 1)], acc[m][n][2 * (i & 1) + 1]};
          a[i] = (f32x2){fma1(u[i][0], LOG2E, -LOG2E), fma1(u[i][1], LOG2E, -LOG2E)}; }
        SCHED();
#pragma unroll
        for (int i = 0; i < 12; i++) { if (ks && i < 7) { rmul(0, i); SCHED(); }
          a[i] = (f32x2){__builtin_amdgcn_exp2f(a[i][0]), __builtin_amdgcn_exp2f(a[i][1])}; SCHED(); }
#pragma unroll
        for (int i = 0; i < 12; i++) a[i] = (f32x2){__builtin_amdgcn_fmed3f(u[i][0], a[i][0], 1.f), __builtin_amdgcn_fmed3f(u[i][1], a[i][1], 1.f)};
        SCHED();
#pragma unroll
        for (int i = 0; i < 12; i++) a[i] = (f32x2){add1(a[i][0], 1e-6f), add1(a[i][1], 1e-6f)};
        SCHED();
#pragma unroll
        for (int i = 0; i < 12; i++) { T[ks][i < 4 ? 4 : (i >> 2) - 1][i & 3] = pk(a[i][0], a[i][1]); SCHED();
          if (ks && i >= 5 && i < 8) { rmul(1, i - 1); SCHED(); } if (ks && (i == 9 || i == 10)) { rmul(1, 2 * (10 - i)); SCHED(); } }
      }
      rmul(1, 3); rmul(1, 1);
      SCHED();
      f32x4 M23, M456;
#pragma unroll
      for (int i = 0; i < 4; i++) {
        M23[i] = (q4 & 1) ? R[3][i] : R[2][i];
        const float r45 = (g == 0) ? R[4][i] : R[5][i];
        M456[i] = __uint_as_float(__builtin_amdgcn_permlane32_swap(__float_as_uint(r45), __float_as_uint(R[6][i]), false, false)[0]);
      }
      f32x4 k0, k1;
      const unsigned aR = L + G_RED + lw * 2048 + lane * 16, aR2 = L + G_RED + (lw ^ 2) * 2048 + lane * 16;
      if (rg) { k0 = M23; k1 = M456; st128(aR, 0, __builtin_bit_cast(u32x4, R[0])); st128(aR, 1024, __builtin_bit_cast(u32x4, R[1])); }
      else { k0 = R[0]; k1 = R[1]; st128(aR, 0, __builtin_bit_cast(u32x4, M23)); st128(aR, 1024, __builtin_bit_cast(u32x4, M456)); }
      if (!__all(fv == tag)) { wait_flags(ws, fk, tag); }
      asm volatile("" ::: "memory");
      *(volatile LAS unsigned*)(size_t)(L + L_PAIR + lw * 4) = 1u;
#pragma unroll 1
      for (int it = 0; it < (1 << 22) && !__all(*(volatile LAS unsigned*)(size_t)(L + L_PAIR + (lane & 1 ? 3 : lw ^ 2) * 4)); it++) {}
      asm volatile("" ::: "memory");
      { const f32x4 o0 = __builtin_bit_cast(f32x4, ld128(aR2, 0)), o1 = __builtin_bit_cast(f32x4, ld128(aR2, 1024));
#pragma unroll
        for (int i = 0; i < 4; i++) { k0[i] = add1(k0[i], o0[i]); k1[i] = add1(k1[i], o1[i]); } }
      gst128c(rws, lane * 16 + WS_PART + (cg * 16 + rb) * PRS + irow * 1024, (u32x4){pk(k0[0], k0[1]), pk(k0[2], k0[3]), pk(k1[0], k1[1]), pk(k1[2], k1[3])});
      SCHED();
      if (wv == 7 && lane == 0) set_flag(ws, FL_G + rb * 8 + cg, tag);
      SCHED();
      u32x4 pv[4][4];
      wo_groups(10, 10);
      const unsigned pb = WS_PART + cg * 16 * PRS + irow * 1024, gr = (rb >> 2) ^ 3;
      const i32x4 sws = make_srd(ws, WS_BYTES);
#pragma unroll 1
      for (int it = 0; it < (1 << 20) && !(prog() & 4u); it++) __builtin_amdgcn_s_sleep(1);
      asm volatile("" ::: "memory");
      wo_groups(10, 20);
      SCHED();
      f32x4 sum[2];
      auto reduce = [&]() {
#pragma unroll
        for (int i = 0; i < 8; i++)
#pragma unroll
          for (int bp = 0; bp < 2; bp++) { sum[bp] = mfma16(sel[i & 1], pv[i >> 1][2 * bp + (i & 1)], i ? sum[bp] : (f32x4){0, 0, 0, 0}); SCHED(); } };
#pragma unroll
      for (int i = 0; i < 16; i++) { pv[i >> 2][i & 3] = gld128c(rws, pvo + (i & 3) * 256, pb + (gr ^ (i >> 2)) * 4 * PRS); SCHED(); }
      if (wv == 7) fv = ld_flag(ws, fo);
      SCHED();
      reduce();
      const bool oset = __all(fv == tag);
      SCHED();
      if (__builtin_amdgcn_ballot_w64(__builtin_isunordered(sum[0][0], sum[1][0]))) {
              unsigned pend = 15;
#pragma unroll 1
        for (int it = 0; it < SWEEPS; it++) {
#pragma unroll
          for (int rp = 0; rp < 4; rp++) if ((pend >> rp & 1) && none_empty(mx4(mx4(mx4(mx4(0, pv[rp][0]), pv[rp][1]), pv[rp][2]), pv[rp][3]))) pend &= ~(1u << rp);
          if (!pend) break;
        #define GRP(r, s, a, b, c, d) "s_bitcmp1_b32 %18, " #r "\n\ts_cbranch_scc0 " #r "f\n\tbuffer_load_dwordx4 %" #a ", %16, %17, %" #s " offen sc1\n\tbuffer_load_dwordx4 %" #b ", %16, %17, %" #s " offen offset:256 sc1\n\t" \
                              "buffer_load_dwordx4 %" #c ", %16, %17, %" #s " offen offset:512 sc1\n\tbuffer_load_dwordx4 %" #d ", %16, %17, %" #s " offen offset:768 sc1\n" #r ":\n\t"
        asm volatile("s_nop 4\n\t" GRP(0, 19, 0, 1, 2, 3) GRP(1, 20, 4, 5, 6, 7) GRP(2, 21, 8, 9, 10, 11) GRP(3, 22, 12, 13, 14, 15) "s_waitcnt vmcnt(0)"
                     : "+v"(pv[0][0]), "+v"(pv[0][1]), "+v"(pv[0][2]), "+v"(pv[0][3]), "+v"(pv[1][0]), "+v"(pv[1][1]), "+v"(pv[1][2]), "+v"(pv[1][3]),
                       "+v"(pv[2][0]), "+v"(pv[2][1]), "+v"(pv[2][2]), "+v"(pv[2][3]), "+v"(pv[3][0]), "+v"(pv[3][1]), "+v"(pv[3][2]), "+v"(pv[3][3])
                     : "v"(pvo), "s"(sws), "s"(pend), "s"(pb + gr * 4 * PRS), "s"(pb + (gr ^ 1) * 4 * PRS), "s"(pb + (gr ^ 2) * 4 * PRS), "s"(pb + (gr ^ 3) * 4 * PRS) : "scc", "memory");
#undef GRP
        }
        reduce();
      }
      SCHED();
      {
#pragma unroll
        for (int bp = 0; bp < 2; bp++) st64(aS, bp * 512, (u32x2){pk(sum[bp][0], sum[bp][1]), pk(sum[bp][2], sum[bp][3])}); }
      auto o_ok = [&]() { if (lane == 0) __hip_atomic_fetch_add((LAS unsigned*)(size_t)(L + L_DONE), 0x8000u, __ATOMIC_RELAXED, __HIP_MEMORY_SCOPE_WORKGROUP); };
      if (wv == 7 && oset) o_ok();
      SCHED();
      WAITL0(); BAR();
      SCHED();
      if (!oset) { wait_flags(ws, fo, tag); o_ok(); }
      __builtin_amdgcn_s_setprio(0);
      SCHED();
      const unsigned so = WS_O + (rt0 + 2 * lw) * OS, sc = L + (lw < 3 ? G_OUT + lw * 5120 : 30720), sp_[2] = {so + h1 * 10240, so + (h1 ^ 1) * 10240};
      unsigned lo[2] = {lane * 16u, lane * 16u + OS}, sw_ = sc + r16 * 160 + g * 8, sr = sc + lane * 16, lko = (((lane >> 1) & 3) ^ (2 * h1)) * 5120 + (lane & 1) * OS;
      PIN(sw_); PIN(sr); PIN(lo[0]); PIN(lo[1]); PIN(lko);
      WAITV(0); arrive(); wait_cnt(8);
      __builtin_amdgcn_s_sleep(6);
      f32x4 oa[2][5];
      unsigned aW[3][3][2], go[5];
#pragma unroll 1
      for (int pass = 0; pass < 3; pass++) {
        u32x2 wf[3][5][2]; u32x4 of[6][2];
        auto look = [&]() { asm volatile("" ::: "memory"); return __builtin_amdgcn_raw_buffer_load_b32(rws, lko, so + 4096 + 1020, SC1); };
        auto await = [&](unsigned mask) {
#pragma unroll 1
          for (int it = 0; it < SWEEPS && (__builtin_amdgcn_ballot_w64(look() == EMPTY) & mask); it++) {}
          asm volatile("" ::: "memory"); };
        if (pass == 2) {
#pragma unroll 1
          for (int it = 0; it < SWEEPS; it++) { unsigned m = 0; asm volatile("" ::: "memory");
#pragma unroll 4
            for (int i = 0; i < 20; i++) m = mx4(mx4(m, gld128c(rws, lo[0], so + i * 1024)), gld128c(rws, lo[1], so + i * 1024));
            if (none_empty(m)) break; }
        }
        asm volatile("" ::: "memory");
        SCHED();
        auto rdo = [&](int ks, int n, int h) { const int p = ks / 5, f = ks % 5;
          wf[ks % 3][n][h] = ldtr(aW[(p + 1) >> 1][f < 4 ? h : 2][n & 1], (p == 2 ? WOP[2] - WOP[1] : 0) + (f < 4 ? f * 40 + 20 * h : 80 * h) * 192 + (n >> 1) * 64); };
        auto ldo = [&](int ks, int m) { of[ks % 6][m] = gld128c(rws, lo[m] + (ks % 10 & 3) * 1024, sp_[ks / 10] + (ks % 10 >> 2) * 4096); };
        unsigned lk; bool early = false;
#pragma unroll 1
        for (int it = 0; it < SWEEPS; it++) {
          lk = look();
          SCHED();
#pragma unroll
          for (int ks = 0; ks < 5; ks++) { ldo(ks, 0); SCHED(); ldo(ks, 1); SCHED(); }
          int ln = lane; PIN(ln);
          { const int lane = ln, g = ln >> 4, q4 = (ln >> 2) & 3, p4 = ln & 3;
#pragma unroll
            for (int e = 0; e < 2; e++)
#pragma unroll
              for (int r = 0; r < 3; r++) {
                aW[2][r][e] = L + WOP[3] + (r < 2 ? 4 * g + q4 : 20 * q4 + 16 + (g ^ (q4 & 2))) * 192 + ((dl + 32 * e + 8 * p4) ^ (((r < 2 ? g + r : q4) & 1) << 5));
                aW[0][r][e] = aW[2][r][e] + WOP[0] - WOP[3]; aW[1][r][e] = aW[2][r][e] + WOP[1] - WOP[3]; PIN(aW[0][r][e]); PIN(aW[1][r][e]); PIN(aW[2][r][e]); }
#pragma unroll
            for (int u = 0; u < 5; u++) { const unsigned c = lane + 64 * u, row = (c * 6554) >> 16; go[u] = row * 2400 + c * 16 + (rt0 + 2 * lw) * 16 * 2560 + 1280 + cg * 160; PIN(go[u]); }
          }
          SCHED();
#pragma unroll
          for (int i = 0; i < 10; i++) { rdo(i / 5, i % 5, 0); rdo(i / 5, i % 5, 1); }
          SCHED();
          if (!(__builtin_amdgcn_ballot_w64(lk == EMPTY) & 0xf)) break;
                  }
        SCHED();
#pragma unroll
        for (int ks = 0; ks < 20; ks++) {
          if (ks == 1) lk = look();
          if (ks == 5 && (__builtin_amdgcn_ballot_w64(__builtin_isunordered(oa[0][0][0], oa[1][0][0])) && pass == 0)) { early = true; break; }
          if (ks == 5 && (__builtin_amdgcn_ballot_w64(lk == EMPTY) & 0xf0)) { await(0xf0); }
#pragma unroll
          for (int n = 0; n < 5; n++) {
            if (n == 0) {
              if (ks >= 1 && ks < 5) __builtin_amdgcn_s_waitcnt(0x0a79); else if (ks < 16) __builtin_amdgcn_s_waitcnt(0x0a78); else if (ks == 16) __builtin_amdgcn_s_waitcnt(0x0a76);
              else if (ks == 17) __builtin_amdgcn_s_waitcnt(0x0a74); else if (ks == 18) __builtin_amdgcn_s_waitcnt(0x0a72); else __builtin_amdgcn_s_waitcnt(0x0070); }
            if (ks % 5 == 4 && n == 0) {
              const u32x4 e = of[ks % 6][0], o = of[ks % 6][1];
              of[ks % 6][0] = (u32x4){e[0], e[1], o[0], o[1]}; of[ks % 6][1] = (u32x4){e[2], e[3], o[2], o[3]};
              asm volatile("s_nop 1" : "+v"(of[ks % 6][0]), "+v"(of[ks % 6][1])); }
            if (ks < 15 && (n == 1 || n == 4)) ldo(ks + 5, n >> 2);
            SCHED();
            const u32x4 w = cat(wf[ks % 3][n][0], wf[ks % 3][n][1]);
            if (ks == 0) MFMA_NEW(oa[0][n], w, of[ks % 6][0]); else MFMA_ACC(oa[0][n], w, of[ks % 6][0]);
            SCHED();
            if (ks < 18) rdo(ks + 2, n, 0);
            SCHED();
            if (ks == 0) MFMA_NEW(oa[1][n], w, of[ks % 6][1]); else MFMA_ACC(oa[1][n], w, of[ks % 6][1]);
            SCHED();
            if (ks < 18) rdo(ks + 2, n, 1);
            SCHED();
          }
        }
        if (early) continue;
        asm volatile("s_nop 7" : "+v"(oa[0][0]), "+v"(oa[0][1]), "+v"(oa[0][2]), "+v"(oa[0][3]), "+v"(oa[0][4]), "+v"(oa[1][0]), "+v"(oa[1][1]), "+v"(oa[1][2]), "+v"(oa[1][3]), "+v"(oa[1][4]));
        if (!(__builtin_amdgcn_ballot_w64(__builtin_isunordered(oa[0][0][0], oa[1][0][0])))) break;
        if (pass == 0) pass = 1;
      }
      SCHED();
#pragma unroll
      for (int m = 0; m < 2; m++)
#pragma unroll
        for (int n = 0; n < 5; n++) { st64(sw_, m * 2560 + n * 32, (u32x2){pk(oa[m][n][0], oa[m][n][1]), pk(oa[m][n][2], oa[m][n][3])}); SCHED(); }
      { u32x4 r[5];
#pragma unroll
        for (int u = 0; u < 5; u++) r[u] = ld128(sr, u * 1024);
#pragma unroll
        for (int u = 0; u < 5; u++) __builtin_amdgcn_raw_buffer_store_b128(r[u], ro, go[u], 0, 0); }
    }
  } else {
    const int ch0 = 320 * hf + 5 * ai;
    const rsrc_t rh = mk(Ah, 640 * 4096);
    u32x4 idl, idh, dm, bw[3][2];
    { const unsigned hm = (g & 1) == (r16 >> 3) ? 0xffffu << ((r16 & 1) * 16) : 0u, one = g < 2 ? 0x3f803f80u : 0u;
#pragma unroll
      for (int i = 0; i < 4; i++) { dm[i] = ((r16 >> 1) & 3) == i ? hm : 0u; idl[i] = dm[i] & one; idh[i] = dm[i] & (one ^ 0x3f803f80u); } }
    unsigned aK[5];
#pragma unroll
    for (int i = 0; i < 5; i++) { const int row = 8 * q4 + 3 - p4 + (i < 4 ? 4 + 4 * (i >> 1) - 2 * (i & 1) - (g >> 1) : 4 * (g >> 1));
      aK[i] = L + (i < 4 ? A_K : A_V) + row * 256 + ((((row >> 1) & 12) ^ (row & 3) ^ (g & 1) ^ (2 * wv)) << 4); PIN(aK[i]); }
    unsigned ko = WS_KVT + hf * 256 * KS + (255 - 4 * ai - g) * KS + r16 * 16 + wv * 256, hpo = (tid & 255) ? ch0 * 4096 + tid * 16 - 8 : 0xffff0000u;
    PIN(ko); PIN(hpo);
    if (wv == 7) { set_warm(); PIN(va[0]); PIN(va[1]); }
    const int chl = wv < 4 ? wv : 4;
    const float Dc = __uint_as_float((*(const __attribute__((address_space(4))) unsigned*)(AD + ((ch0 + chl) & ~1)) >> (((ch0 + chl) & 1) * 16)) << 16);
    if (tid < 6) *(LAS unsigned*)(size_t)(L + L_DONE + tid * 4) = 0;
    asm volatile("s_waitcnt vmcnt(6)" : "+v"(wk), "+v"(dm), "+v"(idl), "+v"(idh)::"memory");
    BAR();
    u32x4 hv[3]; u32x2 hp[3];
    auto load_h = [&]() {
#pragma unroll
      for (int u = 0; u < 3; u++) if (u < 2 || wv < 4) {
        hv[u] = gld128(rh, tid * 16, ch0 * 4096 + u * 8192);
        hp[u] = __builtin_amdgcn_raw_buffer_load_b64(rh, hpo, u * 8192, 0); } };
    auto warm_wo = [&]() { const i32x4 so_ = make_srd(Bo, 640 * 640 * 2);
      const unsigned r = (lane * 52) >> 8, vq = (r * 1280 + (lane - 5 * r) * 128) | (lane < 50 ? 0u : 0xf0000000u);
#pragma unroll
      for (int i = 0; i < 4; i++) touch(so_, vq, (i * 160 + 10 * j) * 1280 + (xcd & 1) * 640, L + L_DUMMY); };
    auto warm_wq = [&]() { const unsigned r = (lane * 52) >> 8, vq = (r * 3840 + (lane - 5 * r) * 128) | (lane < 50 ? 0u : 0xf0000000u);
#pragma unroll
      for (int i = 0; i < 4; i++) touch(sw, vq, (i * 160 + 10 * j) * 3840 + (xcd & 1) * 640, L + L_DUMMY); };
    if (wv == 7) {
#pragma unroll
      for (int s = 7; s < 10; s++) warm(s);
    }
#pragma unroll
    for (int u = 0; u < 3; u++)
#pragma unroll
      for (int e = 0; e < 2; e++) { const unsigned w = __builtin_amdgcn_ds_bpermute((16 * u + r16) * 4, wk[e]);
        bw[u][e] = dm & __builtin_amdgcn_perm(w, w, g < 2 ? 0x01000100u : 0x03020302u); PIN(bw[u][e]); }
    SCHED();
    if (wv == 7) WAITV(6); else WAITV(0);
    SCHED(); BAR();
    SCHED();
    auto kvt = [&](int nt) {
      u32x4 fr[3][5]; f32x4 k0[3], k1[3], v0[3], v1[3];
#pragma unroll
      for (int u = 0; u < nt; u++)
#pragma unroll
        for (int i = 0; i < 5; i++) { fr[u][i] = ld128(aK[i], (i < 4 ? KP : VP) * u); SCHED(); }
      SCHED();
#pragma unroll
      for (int u = 0; u <= nt; u++) {
        if (u < nt) { const f32x4 z = {0, 0, 0, 0};
          k0[u] = mfma16(fr[u][0], bw[u][0], z); SCHED(); k1[u] = mfma16(fr[u][2], bw[u][0], z); SCHED();
          k0[u] = mfma16(fr[u][1], bw[u][1], k0[u]); SCHED(); k1[u] = mfma16(fr[u][3], bw[u][1], k1[u]); SCHED();
          v0[u] = mfma16(fr[u][4], idl, z); SCHED(); v1[u] = mfma16(fr[u][4], idh, z); SCHED(); }
        if (u > 0) { const int w = u - 1;
          gst128c(rws, ko + w * 2048, (u32x4){pk(mul1(k1[w][0], v1[w][0]), mul1(k1[w][1], v1[w][1])), pk(mul1(k1[w][2], v1[w][2]), mul1(k1[w][3], v1[w][3])),
                                              pk(mul1(k0[w][0], v0[w][0]), mul1(k0[w][1], v0[w][1])), pk(mul1(k0[w][2], v0[w][2]), mul1(k0[w][3], v0[w][3]))}); SCHED(); }
      } };
    if (wv < 4) kvt(3);
    else { kvt(2); gst128c(rws, WS_PART + (cg * 16 + rb) * PRS + lw * 1024 + lane * 16, emp); }
    SCHED();
    unsigned xo, xa, ha = L + (tid >> 8) * HCH + 384 + (tid & 255) * 16;
    { const unsigned blk = (tid * 13108) >> 16, ch = tid - 5 * blk; xo = blk * KS + ch * 16; xa = L + A_X + ch * XCH + blk * 16; }
    unsigned aH = L + lw * HCH + (3 - p4) * HCP + (64 * q4 + 8 * g) * 2;
    const int bc = 8 * (r16 & 7) + 4 * (r16 >> 3);
    unsigned aXr = L + A_X + lw * XCH + (1852 - 1792 - bc + 8 * g) * 2;
    unsigned aE = L + A_X + chl * XCH + (2044 - 1792 - 64 * g - bc) * 2;
    unsigned aH5 = aH + A_H4 - lw * HCH + 128 * lw, aX5 = aXr + (4 - lw) * XCH + 128 * lw, aD = L + A_DMP + lw * 2048 + lane * 16, aF = L + L_DONE + lw * 4, aC = L + L_CNT;
    unsigned qo = (tid >> 4) * 1280 + r16 * 16;
    PIN(xo); PIN(xa); PIN(ha); PIN(aH); PIN(aXr); PIN(aE); PIN(aH5); PIN(aX5); PIN(aD); PIN(aF); PIN(aC); PIN(qo);
    auto hcopy = [&]() {
#pragma unroll
      for (int u = 0; u < 3; u++) if (u < 2 || wv < 4) {
        const unsigned e0 = __builtin_amdgcn_alignbyte(hp[u][1], hp[u][0], 2), e1 = __builtin_amdgcn_alignbyte(hv[u][0], hp[u][1], 2), e2 = __builtin_amdgcn_alignbyte(hv[u][1], hv[u][0], 2),
                       e3 = __builtin_amdgcn_alignbyte(hv[u][2], hv[u][1], 2), e4 = __builtin_amdgcn_alignbyte(hv[u][3], hv[u][2], 2);
        const unsigned ad = u < 2 ? ha : ha + A_H4; const int o = u == 1 ? 2 * HCH : 0;
        st128(ad, o, hv[u]); st128(ad, o + HCP, (u32x4){e1, e2, e3, e4}); st128(ad, o + 2 * HCP, (u32x4){hp[u][1], hv[u][0], hv[u][1], hv[u][2]}); st128(ad, o + 3 * HCP, (u32x4){e0, e1, e2, e3}); } };
    WAITV(0);
    BAR();
    if (wv == 7 && lane == 0) set_flag(ws, FL_K + hf * 64 + ai, tag);
    SCHED();
    auto reset_o = [&]() { if (wv == 5 || wv == 6) { const unsigned c2 = WS_O + (hf * 64 + ai) * OS + (wv - 5) * 10240 + lane * 16;
#pragma unroll
        for (int i = 0; i < 10; i++) gst128c(rws, c2 + i * 1024, emp); } };
    load_h();
    if (wv == 6) { warm_wq(); warm_wo(); }
    if ((tid & 31) < 24) { st128(L + (tid >> 5) * HCP + (tid & 31) * 16, 0, (u32x4){0, 0, 0, 0}); if (wv < 2) st128(L + A_H4 + (tid >> 5) * HCP + (tid & 31) * 16, 0, (u32x4){0, 0, 0, 0}); }
    else if (tid < 160) st128(L + A_X + (tid >> 5) * XCH + 4096 + (tid & 7) * 16, 0, (u32x4){0, 0, 0, 0});
    if (wv < 6) hcopy();
    if (wv == 7) wait_flags(ws, FL_K + hf * 64 + lane, tag);
    SCHED();
    WAITL0(); BAR();
    SCHED();
    u32x4 F[2][8];
    if (wv < 5) { u32x4 xv[4]; const int xr = ai & 3;
#pragma unroll
      for (int u = 0; u < 4; u++) xv[u] = gld128c(rws, xo, WS_KVT + hf * 256 * KS + ai * 80 + (u ^ xr) * 64 * KS);
      SCHED();
      if (wv < 4)
#pragma unroll
        for (int t = 0; t < 8; t++) F[0][t] = ld128(aH5, 512 * t);
      SCHED();
#pragma unroll
      for (int u = 0; u < 4; u++) st128(xa + (u ^ xr) * 1024, 0, xv[u]);
    } else {
      if (wv > 5) hcopy(); else __builtin_amdgcn_s_sleep(4);
      reset_o();
    }
    auto resets = [&]() { const unsigned c1 = WS_YT + (ch0 + lw) * YS + lane * 16, c3 = WS_YT + (ch0 + 4) * YS + lw * 1024 + lane * 16;
#pragma unroll
      for (int i = 0; i < 5; i++) gst128c(rws, i < 4 ? c1 + i * 1024 : c3, emp); };
    const rsrc_t rq = mk(Aq, 2048 * 1280);
    u32x4 qv[3];
    unsigned qo2 = (qo + 512) | (r16 & 8) << 28; PIN(qo2);
    auto load_q = [&]() {
#pragma unroll
      for (int u = 0; u < 3; u++) qv[u] = gld128(rq, u < 2 ? qo + u * 256 : qo2, 32 * ai * 1280 + hf * 640); };
    unsigned yo = WS_YT + (ch0 + chl) * YS + 128 * g + (r16 & 7) * 16 + (r16 >> 3) * 512; PIN(yo);
    auto put_y = [&](f32x4 a, f32x4 c2, unsigned off) {
      const unsigned a0_ = pk(a[0], a[1]), a1_ = pk(a[2], a[3]), b0 = pk(c2[0], c2[1]), b1 = pk(c2[2], c2[3]);
      gst128c(rws, yo + off, (u32x4){(unsigned)__builtin_amdgcn_update_dpp(a0_, b0, 0x128, 0xf, 0xc, false), (unsigned)__builtin_amdgcn_update_dpp(a1_, b1, 0x128, 0xf, 0xc, false),
                                     (unsigned)__builtin_amdgcn_update_dpp(b0, a0_, 0x128, 0xf, 0x3, false), (unsigned)__builtin_amdgcn_update_dpp(b1, a1_, 0x128, 0xf, 0x3, false)}); };
    WAITL0(); BAR();
    SCHED();
    if (wv < 4) {
      f32x4 acc[8], ac5[8];
      u32x2 G[2][8][2], xe[8];
      unsigned cv; f32x4 xs[8];
#pragma unroll
      for (int t = 0; t < 8; t++) { G[0][t][0] = ld64(aX5, 512 * (7 - t)); SCHED(); G[0][t][1] = ld64(aX5, 512 * (7 - t) + 8); SCHED(); }
      auto step = [&](f32x4* ac, int cur, unsigned nh, unsigned nx, int off, int mode) {
        int cnt = 0;
#pragma unroll
        for (int t = 0; t < 8; t++)
#pragma unroll
          for (int p = 0; p <= t; p++) {
            const u32x4 gg = cat(G[cur][t - p][0], G[cur][t - p][1]);
            if (p == 0 && (mode & 1)) MFMA_NEW(ac[t], F[cur][p], gg); else MFMA_ACC(ac[t], F[cur][p], gg);
            SCHED();
            if (cnt < 24 && !(mode & 32)) { const int i = cnt / 3, w = cnt % 3;
              if (w == 0) F[1 - cur][i] = ld128(nh, 512 * i + off); else G[1 - cur][i][w - 1] = ld64(nx, 512 * (7 - i) + off + 8 * (w - 1)); }
            if ((mode & 6) && cnt >= 24 && cnt % 3 == 0) { const int d = (cnt - 24) / 3 + (mode & 4); st128(aD, (d >> 1) * 8192 + (d & 1) * 1024, __builtin_bit_cast(u32x4, ac5[d])); }
            if ((mode & 4) && cnt == 35) *(volatile LAS unsigned*)(size_t)aF = 1u;
            if ((mode & 8) && cnt == 25) { aH += 128; PIN(aH); }
            if ((mode & 8) && cnt == 26) { aXr += 128; PIN(aXr); }
            if ((mode & 16) && cnt >= 24 && cnt < 32) xe[cnt - 24] = ld64(aE, 512 * (31 - cnt));
            if ((mode & 16) && cnt == 32) cv = *(volatile LAS unsigned*)(size_t)aC;
            if (mode & 32) {
              const int e = cnt & 3, tt = cnt >> 2;
              if (cnt < 32) { unsigned v = e & 1 ? xe[tt][1 - (e >> 1)] << 16 : xe[tt][1 - (e >> 1)] & 0xffff0000u; PIN(v); xs[tt][e] = __uint_as_float(v); } }
            if (cnt == 34 && !(mode & 32)) { if (mode & 6) WAITL(4); else WAITL0(); }
            cnt++;
            SCHED();
          }
      };
      step(ac5, 0, aH5, aX5, 64, 1); step(ac5, 1, aH, aXr, 0, 0);
      step(acc, 0, aH, aXr, 64, 1 | 2); step(acc, 1, aH, aXr, 128, 4 | 8);
      load_q(); SCHED();
#pragma unroll 1
      for (int r = 0; r < 2; r++) { step(acc, 0, aH, aXr, 64, 0); step(acc, 1, aH, aXr, 128, 8); }
      step(acc, 0, aH, aXr, 64, 16); step(acc, 1, aH, aXr, 0, 32);
      SCHED();
      if (__builtin_amdgcn_readfirstlane(cv) < 8) wait_cnt(8);
#pragma unroll
      for (int t = 0; t < 8; t += 2) { if (t == 6) asm volatile("" : "+v"(acc[6]), "+v"(acc[7]));
        put_y(acc[t] + Dc * xs[t], acc[t + 1] + Dc * xs[t + 1], 512 * t); SCHED(); }
    } else {
      __builtin_amdgcn_s_setprio(1);
      WAITV(0); arrive(); wait_cnt(4);
      if (wv == 7 && lane == 0) set_flag(ws, FL_O + hf * 64 + ai, tag);
      resets();
      WAITV(0); arrive(); wait_cnt(8);
      if (wv == 7 && lane == 0) set_flag(ws, FL_A + hf * 64 + ai, tag);
#pragma unroll 1
      for (int it = 0; it < (1 << 20) && !__all(*(volatile LAS unsigned*)(size_t)(L + L_DONE + p4 * 4) == 1u); it++) __builtin_amdgcn_s_sleep(2);
      asm volatile("" ::: "memory");
      { const unsigned rb_ = L + A_DMP + lw * 8192 + lane * 16;
        f32x4 sm[2];
#pragma unroll
        for (int e = 0; e < 2; e++) { sm[e] = __builtin_bit_cast(f32x4, ld128(rb_, e * 1024));
#pragma unroll
          for (int w = 1; w < 4; w++) { const f32x4 o = __builtin_bit_cast(f32x4, ld128(rb_, w * 2048 + e * 1024));
#pragma unroll
            for (int i = 0; i < 4; i++) sm[e][i] = add1(sm[e][i], o[i]); }
          const u32x2 xr = ld64(aE + 512 * (7 - 2 * lw - e), 0);
          sm[e] = (f32x4){fma1(Dc, bhi(xr[1]), sm[e][0]), fma1(Dc, blo(xr[1]), sm[e][1]), fma1(Dc, bhi(xr[0]), sm[e][2]), fma1(Dc, blo(xr[0]), sm[e][3])}; }
        put_y(sm[0], sm[1], 1024 * lw); }
      load_q();
      if (wv == 7) {
#pragma unroll 1
        for (int it = 0; it < (1 << 18); it++) {
          bool ok = ld_flag(ws, FL_A + hf * 64 + lane) == tag;
          if ((xcd | j) == 0) ok = ok & (ld_flag(ws, lane) == tag) & (ld_flag(ws, 64 + lane) == tag) & (ld_flag(ws, 128 + lane) == tag) & (ld_flag(ws, 192 + lane) == tag);
          if (__all(ok)) break;
          __builtin_amdgcn_s_sleep(4);
        }
        asm volatile("" ::: "memory");
        if ((xcd | j) == 0 && lane == 0) __hip_atomic_store((GAS u64*)(ws + WS_EPOCH), tag, __ATOMIC_RELAXED, __HIP_MEMORY_SCOPE_AGENT);
      }
      __builtin_amdgcn_s_setprio(0);
    }
    SCHED();
    WAITV(0);
    WAITL0(); BAR();
    SCHED();
    f32x2 qf[3][4];
    unsigned ya = L + T_SCR + (tid >> 4) * 640 + (wv >> 1) * 32 + r16 * 16, oo[2];
    oo[0] = (tid >> 4) * 2560 + r16 * 16 + 32 * ai * 2560 + hf * 640; oo[1] = (oo[0] + 512) | (r16 & 8) << 28;
    { u32x4 yv[3];
      const unsigned yl = (tid >> 2) * YS + p4 * 16, ys = L + T_SCR + p4 * (8 * 640 + 32) + (tid >> 2) * 2;
#pragma unroll
      for (int u = 0; u < 3; u++) if (u < 2 || wv < 4) yv[u] = gld128c(rws, yl, WS_YT + (320 * hf + 128 * u) * YS + ai * 64);
      SCHED();
#pragma unroll
      for (int u = 0; u < 3; u++)
#pragma unroll
        for (int e = 0; e < 4; e++) { qf[u][e] = (f32x2){blo(qv[u][e]), bhi(qv[u][e])}; PIN(qf[u][e]); }
      PIN(ya); PIN(oo[0]); PIN(oo[1]);
      SCHED();
#pragma unroll
      for (int u = 0; u < 3; u++) if (u < 2 || wv < 4) {
        if (!none_empty(mx4(0, yv[u])))
#pragma unroll 1
        for (int it = 0; it < SWEEPS && !none_empty(mx4(0, yv[u])); it++) { asm volatile("" ::: "memory");
          if (mx4(0, yv[u]) == EMPTY) yv[u] = gld128c(rws, yl, WS_YT + (320 * hf + 128 * u) * YS + ai * 64); }
#pragma unroll
        for (int e = 0; e < 4; e++) { st16(ys, 256 * u + 1280 * e, (u16)yv[u][e]); st16(ys, 256 * u + 1280 * e + 640, (u16)(yv[u][e] >> 16)); }
        SCHED(); }
      SCHED(); }
    WAITL0(); BAR();
    SCHED();
    {
      u32x4 y[3];
#pragma unroll
      for (int u = 0; u < 3; u++) y[u] = ld128(ya, 256 * u);
      SCHED();
#pragma unroll
      for (int u = 0; u < 3; u++) { u32x4 r;
#pragma unroll
        for (int e = 0; e < 4; e++) { const f32x2 t = (f32x2){blo(y[u][e]), bhi(y[u][e])} * qf[u][e]; r[e] = pk(t[0], t[1]); }
        __builtin_amdgcn_raw_buffer_store_b128(r, ro, u < 2 ? oo[0] + u * 256 : oo[1], 0, SC1); } }
  }
}

extern "C" void kernel_launch(void* const* d_in, const int* in_sizes, int n_in, void* d_out, int out_size, void* d_ws, size_t ws_size, hipStream_t stream) {
  fused_kernel<<<256, 512, 0, stream>>>((const u16*)d_in[0], (const u16*)d_in[1], (const u16*)d_in[2], (const u16*)d_in[3], (const u16*)d_in[4], (const u16*)d_in[5],
                                        (const u16*)d_in[6], (const u16*)d_in[7], (const u16*)d_in[8], (u16*)d_out, (char*)d_ws);
}
```
